```python
import torch

def _softmax(x, axis=-1):
    return torch.softmax(x, dim=axis)
def _gelu(x):
    return torch.nn.functional.gelu(x, approximate="tanh")
def _silu(x):
    return torch.nn.functional.silu(x)
def _sigmoid(x):
    return torch.sigmoid(x)
def _layernorm(x, axis=-1, eps=1e-5):
    m = x.mean(dim=axis, keepdim=True)
    v = x.var(dim=axis, keepdim=True, unbiased=False)
    return (x - m) / torch.sqrt(v + eps)
def _scatter_add(tgt, idx, src, axis=0):
    return tgt.clone().index_add_(axis, idx, src)
def _segment_sum(src, seg, n):
    out = torch.zeros((n,) + src.shape[1:], dtype=src.dtype)
    return out.index_add_(0, seg, src)
def _pad(x, pw):
    flat = tuple(v for pair in reversed(pw) for v in pair)
    return torch.nn.functional.pad(x, flat)

kernel_name = 'recurrent_scan__sliding_local_dilated__dense_activation_silu__prompt'

def setup_inputs():

    _w = (lambda a: (lambda t: t.to(torch.bfloat16) if t.is_floating_point() else t)(torch.as_tensor(a)))
    def _A_setup():
        B, S, D = 1, 2048, 640
        return {
            "x": _w((lambda *s: torch.randn(*s, dtype=torch.bfloat16) * 0.1)(B, S, D)),
            "wqkv": _w((lambda *s: torch.randn(*s, dtype=torch.bfloat16) * 0.2)(D, 3 * D)),
            "wo": _w((lambda *s: torch.randn(*s, dtype=torch.bfloat16) * 0.4)(D, D)),
        }
    _A_in = _A_setup()

    def _B_setup():
        B, S, D, F = 1, 2048, 640, 1728
        return {
            "x": _w((lambda *s: torch.randn(*s, dtype=torch.bfloat16) * 0.1)(B, S, D)),
            "w1": _w((lambda *s: torch.randn(*s, dtype=torch.bfloat16) * 3.2)(D, F) * (D ** -0.5)),
            "w2": _w((lambda *s: torch.randn(*s, dtype=torch.bfloat16) * 3.2)(F, D) * (F ** -0.5)),
        }
    _B_in = _B_setup()

    return {
        "A_x": _A_in["x"],
        "A_wqkv": _A_in["wqkv"],
        "A_wo": _A_in["wo"],
        "B_x": _B_in["x"],
        "B_w1": _B_in["w1"],
        "B_w2": _B_in["w2"],
    }

def reference(A_x, A_wqkv, A_wo, B_x, B_w1, B_w2):
    def _A_ref(x, wqkv, wo):
        B, S, D = x.shape
        H = 32; Dh = D // H
        W, dil = 8, 2
        qkv = (lambda x, *d: x.permute(*d))((x @ wqkv).reshape(B, S, 3, H, Dh), 2, 0, 3, 1, 4)
        q, k, v = qkv[0], qkv[1], qkv[2]
        scale = Dh ** -0.5
        pos = torch.arange(S)
        off = torch.arange(W) * dil
        idx = pos[:, None] - off[None, :]
        mask = idx >= 0
        idx_c = torch.where(mask, idx, 0).reshape(-1)
        kg = k[:, :, idx_c, :].reshape(B, H, S, W, Dh)
        vg = v[:, :, idx_c, :].reshape(B, H, S, W, Dh)
        logits = torch.einsum("bhsd,bhswd->bhsw", q, kg) * scale
        logits = torch.where(mask[None, None, :, :], logits, -1e9)
        attn = _softmax(logits, axis=-1)
        o = torch.einsum("bhsw,bhswd->bhsd", attn, vg)
        return (lambda x, *d: x.permute(*d))(o, 0, 2, 1, 3).reshape(B, S, D) @ wo

    def _B_ref(x, w1, w2):
        h = torch.einsum("bsd,df->bsf", x, w1)
        h = _silu(h)
        out = torch.einsum("bsf,fd->bsd", h, w2)
        return out

    x = A_x
    xn = _layernorm(x, axis=-1)
    s = _A_ref(xn, A_wqkv, A_wo)
    g = _silu(_B_ref(xn, B_w1, B_w2))
    return x + s * g
```

```cpp
#include <hip/hip_runtime.h>
#include <hip/hip_bf16.h>
#include <stdint.h>

typedef unsigned short u16;
__device__ __forceinline__ float bf2f(u16 v){ return __uint_as_float(((unsigned)v)<<16); }
__device__ __forceinline__ u16 f2bf(float f){ __hip_bfloat16 b = __float2bfloat16(f); return *(u16*)&b; }

constexpr int S=2048, D=640, F=1728, H=32, DH=20;

__global__ void ln_k(const u16* x, u16* xn){
  int row = blockIdx.x; int t = threadIdx.x;
  float v[10]; float s=0;
  for(int i=0;i<10;i++){ v[i]=bf2f(x[row*D + t + 64*i]); s+=v[i]; }
  for(int o=32;o;o>>=1) s+=__shfl_xor(s,o);
  float m=s/D; float q=0;
  for(int i=0;i<10;i++){ float d=v[i]-m; q+=d*d; }
  for(int o=32;o;o>>=1) q+=__shfl_xor(q,o);
  float r=rsqrtf(q/D+1e-5f);
  for(int i=0;i<10;i++) xn[row*D+t+64*i]=f2bf((v[i]-m)*r);
}
template<int MODE>
__global__ void gemm_k(const u16* A, const u16* B, void* C, int M,int N,int K){
  __shared__ float As[16][17], Bs[16][17];
  int tx=threadIdx.x&15, ty=threadIdx.x>>4;
  int r=blockIdx.y*16+ty, c=blockIdx.x*16+tx;
  float acc=0;
  for(int k0=0;k0<K;k0+=16){
    As[ty][tx]=bf2f(A[r*K+k0+tx]);
    Bs[ty][tx]=bf2f(B[(k0+ty)*N+c]);
    __syncthreads();
    for(int k=0;k<16;k++) acc+=As[ty][k]*Bs[k][tx];
    __syncthreads();
  }
  if(MODE==0) ((u16*)C)[r*N+c]=f2bf(acc);
  else if(MODE==1) ((u16*)C)[r*N+c]=f2bf(acc/(1.f+__expf(-acc)));
  else ((float*)C)[r*N+c]=acc;
}
__global__ void attn_k(const u16* qkv, u16* o){
  int idx=blockIdx.x*blockDim.x+threadIdx.x; if(idx>=S*H) return;
  int s=idx/H, h=idx%H;
  float q[DH]; for(int d=0;d<DH;d++) q[d]=bf2f(qkv[s*3*D+h*DH+d]);
  float l[8]; float mx=-1e30f;
  for(int w=0;w<8;w++){ int p=s-2*w; float a=-1e9f; if(p>=0){ a=0; for(int d=0;d<DH;d++) a+=q[d]*bf2f(qkv[p*3*D+D+h*DH+d]); a*=0.22360679775f;} l[w]=a; mx=fmaxf(mx,a);}
  float sum=0; for(int w=0;w<8;w++){ l[w]=__expf(l[w]-mx); sum+=l[w]; }
  float inv=1.f/sum;
  for(int d=0;d<DH;d++){ float a=0; for(int w=0;w<8;w++){ int p=s-2*w; if(p>=0) a+=l[w]*bf2f(qkv[p*3*D+2*D+h*DH+d]); } o[s*D+h*DH+d]=f2bf(a*inv);}
}
__global__ void fin_k(const u16* x, const float* sacc, const float* gacc, u16* out){
  int i=blockIdx.x*blockDim.x+threadIdx.x; if(i>=S*D) return;
  float g=gacc[i]; g=g/(1.f+__expf(-g));
  out[i]=f2bf(bf2f(x[i])+sacc[i]*g);
}
extern "C" void kernel_launch(void* const* d_in, const int* in_sizes, int n_in,
                              void* d_out, int out_size, void* d_ws, size_t ws_size,
                              hipStream_t stream) {
  const u16* x=(const u16*)d_in[0]; const u16* wqkv=(const u16*)d_in[1]; const u16* wo=(const u16*)d_in[2];
  const u16* w1=(const u16*)d_in[4]; const u16* w2=(const u16*)d_in[5];
  char* ws=(char*)d_ws;
  u16* xn=(u16*)ws; ws+=S*D*2;
  u16* qkv=(u16*)ws; ws+=S*3*D*2;
  u16* h=(u16*)ws; ws+=S*F*2;
  u16* o=(u16*)ws; ws+=S*D*2;
  float* sacc=(float*)ws; ws+=S*D*4;
  float* gacc=(float*)ws; ws+=S*D*4;
  ln_k<<<S,64,0,stream>>>(x,xn);
  gemm_k<0><<<dim3(3*D/16,S/16),256,0,stream>>>(xn,wqkv,qkv,S,3*D,D);
  gemm_k<1><<<dim3(F/16,S/16),256,0,stream>>>(xn,w1,h,S,F,D);
  attn_k<<<S*H/256,256,0,stream>>>(qkv,o);
  gemm_k<2><<<dim3(D/16,S/16),256,0,stream>>>(o,wo,sacc,S,D,D);
  gemm_k<2><<<dim3(D/16,S/16),256,0,stream>>>(h,w2,gacc,S,D,F);
  fin_k<<<S*D/256,256,0,stream>>>(x,sacc,gacc,(u16*)d_out);
}
```

```cpp
#include <hip/hip_runtime.h>
#include <hip/hip_bf16.h>
#include <stdint.h>
#include <utility>
#include <type_traits>

typedef unsigned short u16;
typedef __attribute__((ext_vector_type(8))) __bf16 bf16x8;
typedef __attribute__((ext_vector_type(4))) __bf16 bf16x4;
typedef __attribute__((ext_vector_type(2))) __bf16 bf16x2;
typedef __attribute__((ext_vector_type(4))) float f32x4;
typedef __attribute__((ext_vector_type(2))) float f32x2;
typedef __attribute__((ext_vector_type(4))) unsigned u32x4;
typedef __attribute__((ext_vector_type(2))) unsigned u32x2;
#define LAS __attribute__((address_space(3)))
#define GAS __attribute__((address_space(1)))
typedef __amdgpu_buffer_rsrc_t rsrc_t;

#define FENCE() asm volatile("" ::: "memory")
#define BAR() do { asm volatile("s_waitcnt lgkmcnt(0)" ::: "memory"); __builtin_amdgcn_s_barrier(); FENCE(); } while (0)
#define WAITV(n) asm volatile("s_waitcnt vmcnt(%0)" ::"n"(n) : "memory")
#define WAITL0() asm volatile("s_waitcnt lgkmcnt(0)" ::: "memory")

constexpr int S_ = 2048, D_ = 640, F_ = 1728;
constexpr int NKS = 74;
constexpr int NKS_G = 54;
constexpr size_t WS_ACT = 0;
constexpr size_t ACT_BYTES = 128ull * NKS * 1024;
constexpr size_t WS_QKV = WS_ACT + ACT_BYTES;
constexpr size_t QKV_BYTES = 8ull * 128 * 7680;
constexpr size_t WS_WT = WS_QKV + QKV_BYTES + 4096;
constexpr size_t WT_BYTES = 8ull * NKS * 5120;
constexpr size_t WS_CNT = WS_WT + WT_BYTES;
constexpr size_t WS_END = WS_CNT + 32 * 256;

__device__ __forceinline__ rsrc_t mk_rsrc(const void* p, unsigned bytes) {
  return __builtin_amdgcn_make_buffer_rsrc((void*)p, 0, bytes, 0x00020000);
}
__device__ __forceinline__ u32x4 mk_srd(const void* p, unsigned bytes) {
  const unsigned long long a = (unsigned long long)p;
  return (u32x4){(unsigned)a, (unsigned)(a >> 32) & 0xffffu, bytes, 0x00020000u};
}
__device__ __forceinline__ void dma16(u32x4 srd, char* lds, unsigned voff, unsigned soff) {
  const unsigned la = (unsigned)(uintptr_t)(LAS char*)lds;
  asm volatile("s_mov_b32 m0, %0\n\ts_nop 0\n\tbuffer_load_dwordx4 %1, %2, %3 offen lds"
               :: "s"(la), "v"(voff), "s"(srd), "s"(soff));
}
__device__ __forceinline__ bf16x4 tr_read(const char* p) {
  return __builtin_amdgcn_ds_read_tr16_b64_v4bf16((LAS bf16x4*)p);
}
__device__ __forceinline__ bf16x8 cat8(bf16x4 a, bf16x4 b) {
  return __builtin_shufflevector(a, b, 0, 1, 2, 3, 4, 5, 6, 7);
}
__device__ __forceinline__ unsigned pack2(float a, float b) {
  bf16x2 v = {(__bf16)a, (__bf16)b};
  return __builtin_bit_cast(unsigned, v);
}
__device__ __forceinline__ float silu_f(float y) {
  return y * __builtin_amdgcn_rcpf(1.f + __builtin_amdgcn_exp2f(-1.44269504089f * y));
}

template <class Fn, int... Is>
__device__ __forceinline__ void static_for(Fn& f, std::integer_sequence<int, Is...>) {
  (f(std::integral_constant<int, Is>{}), ...);
}

constexpr int MT = 159;
constexpr int NMT = 13, NNT = 19;
constexpr int K1_XB = 160 * 128;
constexpr int K1_WB = 64 * 384;
constexpr int K1_ST = K1_XB + K1_WB;
constexpr int K1_NSLOT = 3;
constexpr int K1_EPI = K1_NSLOT * K1_ST;
constexpr int K1_LDS = K1_EPI + 160 * 8 + 192 * 4;

__global__ __launch_bounds__(512) void k1_kernel(const u16* __restrict__ x, const u16* __restrict__ wqkv,
                                                 const u16* __restrict__ w1, const u16* __restrict__ w2,
                                                 const u16* __restrict__ wo, char* __restrict__ ws) {
  extern __shared__ __attribute__((aligned(16))) char lds[];
  const int lane = threadIdx.x & 63;
  const int w = __builtin_amdgcn_readfirstlane(threadIdx.x >> 6);
  const int bid = blockIdx.x;
  const int xcd = bid & 7, idx = bid >> 3;
  int t = xcd * 31 + idx;
  const bool spare = (idx == 31) || (t >= NMT * NNT);

  if (bid == 0 && threadIdx.x < 32) {
    *(unsigned*)(ws + WS_CNT + threadIdx.x * 256) = 0u;
  }

  if (spare) {
    const int wk = (idx == 31) ? xcd : 8;
    const u32x4 r2 = mk_srd(w2, F_ * D_ * 2);
    const u32x4 ro = mk_srd(wo, D_ * D_ * 2);
    const rsrc_t rt = mk_rsrc(ws + WS_WT, (unsigned)WT_BYTES);
    unsigned voff[5];
#pragma unroll
    for (int i = 0; i < 5; ++i) {
      const int q = (w * 5 + i) * 64 + lane;
      const int row = q / 80, pc = q % 80;
      const int seg = pc >> 1;
      const int key = (row & 3) | (((row >> 3) & 1) << 2);
      voff[i] = row * 1280 + (((seg ^ key) << 1) | (pc & 1)) * 16;
    }
    const int g = lane >> 4, q4 = (lane & 15) >> 2, p4 = lane & 3;
    const int key = q4 | ((g & 1) << 2);
    const int n_it = (NKS - wk + 8) / 9;
    auto issue = [&](int it) {
      const int ks = wk + it * 9;
      char* dst = lds + (it & 1) * 40960 + w * 5120;
      if (ks < NKS_G) {
#pragma unroll
        for (int i = 0; i < 5; ++i) dma16(r2, dst + i * 1024, voff[i], ks * 40960);
      } else {
#pragma unroll
        for (int i = 0; i < 5; ++i) dma16(ro, dst + i * 1024, voff[i], (ks - NKS_G) * 40960);
      }
    };
    issue(0);
    for (int it = 0; it < n_it; ++it) {
      const int ks = wk + it * 9;
      WAITV(0);
      BAR();
      if (it + 1 < n_it) issue(it + 1);
      const char* src = lds + (it & 1) * 40960;
#pragma unroll
      for (int c = 0; c < 5; ++c) {
        const int seg = (w * 5 + c) ^ key;
        const char* a0 = src + (8 * g + q4) * 1280 + seg * 32 + p4 * 8;
        bf16x4 lo = tr_read(a0);
        bf16x4 hi = tr_read(a0 + 4 * 1280);
        bf16x8 f = cat8(lo, hi);
        __builtin_amdgcn_raw_buffer_store_b128(__builtin_bit_cast(u32x4, f), rt, lane * 16 + c * 1024,
                                               (w * NKS + ks) * 5120, 0);
      }
    }
    return;
  }

  int mt, nt;
  {
    const int band = t / 65;
    const int rem = t - band * 65;
    const int bw = (band == 3) ? 4 : 5;
    mt = rem / bw;
    nt = band * 5 + (rem - mt * bw);
  }
  const int m0 = mt * MT;
  const bool isq = nt < 10;
  const unsigned wstride = isq ? 3840u : 3456u;
  const u32x4 rx = mk_srd(x, S_ * D_ * 2);
  const u32x4 rw = mk_srd(isq ? wqkv : w1, isq ? D_ * 1920 * 2 : D_ * F_ * 2);
  const unsigned wcol0 = (isq ? nt : nt - 10) * 384u;

  if (threadIdx.x < K1_NSLOT * 8) {
    const int sl = threadIdx.x >> 3, pc = threadIdx.x & 7;
    *(u32x4*)(lds + sl * K1_ST + 159 * 128 + pc * 16) = (u32x4){0x3F803F80u, 0x3F803F80u, 0x3F803F80u, 0x3F803F80u};
  }

  const int nx = (w < 4) ? 3 : 2;
  const int xi0 = (w < 4) ? w * 3 : 12 + (w - 4) * 2;
  unsigned xvoff[3], wvoff[3];
#pragma unroll
  for (int i = 0; i < 3; ++i) {
    const int row = (xi0 + i) * 8 + (lane >> 3);
    int grow = m0 + row;
    grow = grow > S_ - 1 ? S_ - 1 : grow;
    xvoff[i] = grow * 1280 + (((lane & 7) ^ ((row >> 1) & 7)) * 16);
    const int q = (w * 3 + i) * 64 + lane;
    const int kr = q / 24, pc = q % 24;
    const int sw = ((kr >> 1) & 1) | (((kr >> 3) & 1) << 1);
    wvoff[i] = kr * wstride + wcol0 + ((((pc >> 1) ^ sw) << 1) | (pc & 1)) * 16;
  }
  const bool last_x_masked = (w == 7);

  auto issue = [&](int st, int sl) {
    char* xb = lds + sl * K1_ST;
    char* wb = xb + K1_XB;
    dma16(rx, xb + (xi0 + 0) * 1024, xvoff[0], st * 128);
    if (last_x_masked) {
      if (lane < 56) dma16(rx, xb + (xi0 + 1) * 1024, xvoff[1], st * 128);
    } else {
      dma16(rx, xb + (xi0 + 1) * 1024, xvoff[1], st * 128);
    }
    if (w < 4) dma16(rx, xb + (xi0 + 2) * 1024, xvoff[2], st * 128);
#pragma unroll
    for (int i = 0; i < 3; ++i) dma16(rw, wb + (w * 3 + i) * 1024, wvoff[i], st * 64 * wstride);
  };

  issue(0, 0);
  issue(1, 1);

  const int mi = w & 1, ni = w >> 1;
  const int m = lane & 15, g = lane >> 4;
  const int q4 = (lane & 15) >> 2, p4 = lane & 3;
  const int xrd0 = (mi * 80 + m) * 128 + ((g ^ (m >> 1)) * 16);
  int wrd[3];
  {
    const int sw = ((q4 >> 1) & 1) | ((g & 1) << 1);
#pragma unroll
    for (int c = 0; c < 3; ++c) wrd[c] = K1_XB + (8 * g + q4) * 384 + (((ni * 3 + c) ^ sw) * 32) + p4 * 8;
  }

  f32x4 acc[5][3];
#pragma unroll
  for (int a = 0; a < 5; ++a)
#pragma unroll
    for (int c = 0; c < 3; ++c) acc[a][c] = (f32x4){0.f, 0.f, 0.f, 0.f};
  float ssum[3] = {0.f, 0.f, 0.f}, ssq[3] = {0.f, 0.f, 0.f};
  const bf16x2 ones2 = {(__bf16)1.0f, (__bf16)1.0f};

  int sl = 0;
  for (int st = 0; st < 10; ++st) {
    if (st < 9) {
      if (w < 4) WAITV(6); else WAITV(5);
    } else {
      WAITV(0);
    }
    BAR();
    if (st + 2 < 10) {
      int s2 = sl + 2; if (s2 >= K1_NSLOT) s2 -= K1_NSLOT;
      issue(st + 2, s2);
    }
    const char* sb = lds + sl * K1_ST;
#pragma unroll
    for (int i = 0; i < 3; ++i) {
      if (i < nx) {
        const u32x4 v = *(const u32x4*)(sb + (xi0 + i) * 1024 + lane * 16);
#pragma unroll
        for (int j = 0; j < 4; ++j) {
          const unsigned uj = v[j];
          const bf16x2 p = __builtin_bit_cast(bf16x2, uj);
          ssum[i] = __builtin_amdgcn_fdot2_f32_bf16(p, ones2, ssum[i], false);
          ssq[i] = __builtin_amdgcn_fdot2_f32_bf16(p, p, ssq[i], false);
        }
      }
    }
#pragma unroll
    for (int ks = 0; ks < 2; ++ks) {
      bf16x8 xf[5], wf[3];
#pragma unroll
      for (int a = 0; a < 5; ++a) xf[a] = *(const bf16x8*)(sb + ((xrd0 ^ (ks * 64)) + a * 2048));
#pragma unroll
      for (int c = 0; c < 3; ++c) {
        const char* p = sb + wrd[c] + ks * 32 * 384;
        wf[c] = cat8(tr_read(p), tr_read(p + 4 * 384));
      }
#pragma unroll
      for (int a = 0; a < 5; ++a)
#pragma unroll
        for (int c = 0; c < 3; ++c)
          acc[a][c] = __builtin_amdgcn_mfma_f32_16x16x32_bf16(wf[c], xf[a], acc[a][c], 0, 0, 0);
    }
    sl = (sl + 1 == K1_NSLOT) ? 0 : sl + 1;
  }

  float* e_stat = (float*)(lds + K1_EPI);
  float* e_cs = (float*)(lds + K1_EPI + 160 * 8);
#pragma unroll
  for (int i = 0; i < 3; ++i) {
    if (i < nx) {
      float s = ssum[i], q = ssq[i];
#pragma unroll
      for (int o = 1; o < 8; o <<= 1) { s += __shfl_xor(s, o); q += __shfl_xor(q, o); }
      const float mean = s * (1.f / D_);
      float var = q * (1.f / D_) - mean * mean;
      var = var > 0.f ? var : 0.f;
      const float rstd = __builtin_amdgcn_rsqf(var + 1e-5f);
      if ((lane & 7) == 0) {
        const int row = (xi0 + i) * 8 + (lane >> 3);
        *(f32x2*)(e_stat + row * 2) = (f32x2){rstd, -mean * rstd};
      }
    }
  }
  if (mi == 1 && m == 15) {
#pragma unroll
    for (int c = 0; c < 3; ++c) *(f32x4*)(e_cs + (ni * 3 + c) * 16 + g * 4) = acc[4][c];
  }
  WAITL0();
  BAR();

  f32x4 cs[3];
#pragma unroll
  for (int c = 0; c < 3; ++c) cs[c] = *(const f32x4*)(e_cs + (ni * 3 + c) * 16 + g * 4);

  const rsrc_t ro = isq ? mk_rsrc(ws + WS_QKV, (unsigned)QKV_BYTES) : mk_rsrc(ws + WS_ACT, (unsigned)ACT_BYTES);
  unsigned coff[3];
#pragma unroll
  for (int c = 0; c < 3; ++c) {
    const int col = (isq ? nt : nt - 10) * 192 + (ni * 3 + c) * 16 + g * 4;
    if (isq) {
      const int sec = col / 640, cc = col - sec * 640;
      const int hg = cc / 80, pc = sec * 20 + (cc - hg * 80) / 4;
      coff[c] = hg * (128 * 7680) + pc * 128;
    } else {
      coff[c] = (col >> 5) * 1024 + ((col >> 3) & 3) * 256 + ((col >> 2) & 1) * 8;
    }
  }
#pragma unroll
  for (int a = 0; a < 5; ++a) {
    const int lrow = mi * 80 + a * 16 + m;
    const int grow = m0 + lrow;
    const f32x2 stt = *(const f32x2*)(e_stat + lrow * 2);
    const bool ok = (lrow < MT) && (grow < S_);
    const unsigned roff = isq ? (grow >> 4) * 7680 + (grow & 15) * 8 : (grow >> 4) * (NKS * 1024) + (grow & 15) * 16;
#pragma unroll
    for (int c = 0; c < 3; ++c) {
      f32x4 y;
#pragma unroll
      for (int r = 0; r < 4; ++r) y[r] = acc[a][c][r] * stt[0] + cs[c][r] * stt[1];
      if (!isq) {
#pragma unroll
        for (int r = 0; r < 4; ++r) y[r] = silu_f(y[r]);
      }
      u32x2 o = {pack2(y[0], y[1]), pack2(y[2], y[3])};
      if (ok) __builtin_amdgcn_raw_buffer_store_b64(o, ro, roff + coff[c], 0, 0);
    }
  }
}

constexpr int K2_ST = 36 * 1024;
constexpr int K2_NSLOT = 3;
constexpr int K2_ATT = K2_NSLOT * K2_ST;
constexpr int K2_LDS = K2_ATT + 40 * 1024;
constexpr int K2_NST = 19;

__device__ __forceinline__ constexpr int k2_ks0(int st) { return st < 14 ? st * 4 : NKS_G + (st - 14) * 4; }
__device__ __forceinline__ constexpr int k2_cnt(int st) { return st >= K2_NST ? 0 : (st == 13 ? 3 : 5); }

__global__ __launch_bounds__(512) void k2_kernel(const u16* __restrict__ x, char* __restrict__ ws,
                                                 u16* __restrict__ out) {
  extern __shared__ __attribute__((aligned(16))) char lds[];
  const int lane = threadIdx.x & 63;
  const int w = __builtin_amdgcn_readfirstlane(threadIdx.x >> 6);
  const int bid = blockIdx.x;
  const int xcd = bid & 7, idx = bid >> 3;
  const int mt = xcd * 4 + (idx >> 3), nt = idx & 7;
  const int rb = w & 3, par = w >> 2;
  const int m = lane & 15, g = lane >> 4;
  const int q4 = (lane & 15) >> 2, p4 = lane & 3;

  const rsrc_t ract = mk_rsrc(ws + WS_ACT, (unsigned)ACT_BYTES);
  const u32x4 sact = mk_srd(ws + WS_ACT, (unsigned)ACT_BYTES);
  const u32x4 rqkv = mk_srd(ws + WS_QKV, (unsigned)QKV_BYTES);
  const u32x4 rwt = mk_srd(ws + WS_WT, (unsigned)WT_BYTES);
  GAS unsigned* cnt = (GAS unsigned*)(ws + WS_CNT + mt * 256);

  {
    char* img = lds + K2_ATT;
    const unsigned base = (nt * 128 + 4 * mt) * 7680;
    const unsigned hbase = (mt == 0) ? base : base - 7680;
    if (w < 7 || lane < 32) dma16(rqkv, img + w * 1024, lane * 16, hbase + w * 1024);
#pragma unroll
    for (int i = 0; i < 4; ++i) {
      const int j = i * 8 + w;
      if (j < 30) dma16(rqkv, img + 7680 + j * 1024, lane * 16, base + j * 1024);
    }
  }
  const rsrc_t rx = mk_rsrc(x, S_ * D_ * 2);
  const unsigned xo = (mt * 64 + rb * 16 + m) * 1280 + (nt * 80 + par * 48 + g * 4) * 2;

  auto issue = [&](auto stc, int sl) {
    constexpr int st = decltype(stc)::value;
    constexpr int ks0 = k2_ks0(st);
    char* sb = lds + sl * K2_ST;
    const unsigned abase = ((mt * 4 + rb) * NKS + ks0 + par) * 1024;
    dma16(sact, sb + (rb * 4 + par) * 1024, lane * 16, abase);
    if (st != 13) dma16(sact, sb + (rb * 4 + par + 2) * 1024, lane * 16, abase + 2048);
    const unsigned bbase = (nt * NKS + ks0) * 5120;
    char* bb = sb + 16384;
    if (st != 13) {
      dma16(rwt, bb + w * 2560, lane * 16, bbase + w * 2560);
      dma16(rwt, bb + w * 2560 + 1024, lane * 16, bbase + w * 2560 + 1024);
      if (lane < 32) dma16(rwt, bb + w * 2560 + 2048, lane * 16, bbase + w * 2560 + 2048);
    } else {
      dma16(rwt, bb + w * 1280, lane * 16, bbase + w * 1280);
      if (lane < 16) dma16(rwt, bb + w * 1280 + 1024, lane * 16, bbase + w * 1280 + 1024);
    }
  };
  issue(std::integral_constant<int, 0>{}, 0);
  issue(std::integral_constant<int, 1>{}, 1);

  WAITV(10);
  BAR();
  {
    const char* img = lds + K2_ATT;
    const int hh = w & 3, b = w >> 2;
    const float sc = 0.22360679775f * 1.44269504089f;
#pragma unroll
    for (int p = 0; p < 2; ++p) {
      const int lq = 16 + 32 * b + 2 * m + p;
      int lp = lq - 32; lp = lp < 0 ? 0 : lp;
      const char* aq = img + (lq >> 4) * 7680 + (lq & 15) * 8 + hh * 640;
      const char* ap = img + (lp >> 4) * 7680 + (lp & 15) * 8 + hh * 640;
      bf16x4 q0 = *(const bf16x4*)(aq + g * 128);
      bf16x4 q1 = *(const bf16x4*)(aq + 4 * 128);
      u32x2 q1u = __builtin_bit_cast(u32x2, q1);
      q1u[0] = g == 0 ? q1u[0] : 0u; q1u[1] = g == 0 ? q1u[1] : 0u;
      const bf16x8 qf = cat8(q0, __builtin_bit_cast(bf16x4, q1u));
      const bf16x8 kc = cat8(*(const bf16x4*)(aq + 2560 + g * 128), *(const bf16x4*)(aq + 2560 + 512));
      const bf16x8 kp = cat8(*(const bf16x4*)(ap + 2560 + g * 128), *(const bf16x4*)(ap + 2560 + 512));
      const f32x4 z4 = {0.f, 0.f, 0.f, 0.f};
      f32x4 sp = __builtin_amdgcn_mfma_f32_16x16x32_bf16(kp, qf, z4, 0, 0, 0);
      f32x4 scur = __builtin_amdgcn_mfma_f32_16x16x32_bf16(kc, qf, z4, 0, 0, 0);
      const int kq = 16 + 32 * b + 2 * (4 * g + q4) + p;
      int kpv = kq - 32; kpv = kpv < 0 ? 0 : kpv;
      const char* vq = img + (kq >> 4) * 7680 + (kq & 15) * 8 + hh * 640 + 5120;
      const char* vp = img + (kpv >> 4) * 7680 + (kpv & 15) * 8 + hh * 640 + 5120;
      const bf16x8 v0 = cat8(tr_read(vp + p4 * 128), tr_read(vq + p4 * 128));
      const bf16x8 v1 = cat8(tr_read(vp + 512), tr_read(vq + 512));
      const bool prev_ok = !(mt == 0 && b == 0);
      float e[8];
      float mx = -3.0e38f;
#pragma unroll
      for (int r = 0; r < 4; ++r) {
        const int jj = 4 * g + r;
        const bool okp = prev_ok && (jj >= m + 9);
        const bool okc = (jj <= m) && (m - jj <= 7);
        e[r] = okp ? sp[r] * sc : -3.0e38f;
        e[4 + r] = okc ? scur[r] * sc : -3.0e38f;
        mx = fmaxf(mx, fmaxf(e[r], e[4 + r]));
      }
      mx = fmaxf(mx, __shfl_xor(mx, 16));
      mx = fmaxf(mx, __shfl_xor(mx, 32));
      float sum = 0.f;
#pragma unroll
      for (int r = 0; r < 8; ++r) { e[r] = __builtin_amdgcn_exp2f(e[r] - mx); sum += e[r]; }
      sum += __shfl_xor(sum, 16);
      sum += __shfl_xor(sum, 32);
      const float inv = __builtin_amdgcn_rcpf(sum);
      u32x4 pu = {pack2(e[0] * inv, e[1] * inv), pack2(e[2] * inv, e[3] * inv), pack2(e[4] * inv, e[5] * inv),
                  pack2(e[6] * inv, e[7] * inv)};
      const bf16x8 pf = __builtin_bit_cast(bf16x8, pu);
      const f32x4 o0 = __builtin_amdgcn_mfma_f32_16x16x32_bf16(v0, pf, z4, 0, 0, 0);
      const f32x4 o1 = __builtin_amdgcn_mfma_f32_16x16x32_bf16(v1, pf, z4, 0, 0, 0);
      const int R = mt * 64 + 32 * b + 2 * m + p;
      const unsigned rowoff = (R >> 4) * (NKS * 1024) + (R & 15) * 16 + NKS_G * 1024;
      const int P0 = 20 * nt + 5 * hh + g, P1 = 20 * nt + 5 * hh + 4;
      const unsigned a0 = rowoff + (P0 >> 3) * 1024 + ((P0 >> 1) & 3) * 256 + (P0 & 1) * 8;
      const unsigned a1 = rowoff + (P1 >> 3) * 1024 + ((P1 >> 1) & 3) * 256 + (P1 & 1) * 8;
      u32x2 s0 = {pack2(o0[0], o0[1]), pack2(o0[2], o0[3])};
      u32x2 s1 = {pack2(o1[0], o1[1]), pack2(o1[2], o1[3])};
      __builtin_amdgcn_raw_buffer_store_b64(s0, ract, a0, 0, 16);
      if (g == 0) __builtin_amdgcn_raw_buffer_store_b64(s1, ract, a1, 0, 16);
    }
  }
  WAITV(0);
  BAR();
  if (threadIdx.x == 0) __hip_atomic_fetch_add(cnt, 1u, __ATOMIC_RELAXED, __HIP_MEMORY_SCOPE_AGENT);

  f32x4 accg[5], accs[5];
#pragma unroll
  for (int c = 0; c < 5; ++c) { accg[c] = (f32x4){0.f, 0.f, 0.f, 0.f}; accs[c] = (f32x4){0.f, 0.f, 0.f, 0.f}; }

  auto stage = [&](auto stc) {
    constexpr int st = decltype(stc)::value;
    constexpr int sl = st % K2_NSLOT;
    if constexpr (st == 12) {
      if (w == 0) {
        unsigned spins = 0;
        while (__builtin_amdgcn_readfirstlane(__hip_atomic_load(cnt, __ATOMIC_RELAXED, __HIP_MEMORY_SCOPE_AGENT)) < 8u) {
          __builtin_amdgcn_s_sleep(1);
          if (++spins > (1u << 22)) break;
        }
        __builtin_amdgcn_fence(__ATOMIC_ACQUIRE, "agent");
        WAITV(0);
      }
    }
    if constexpr (st > 0) {
      WAITV(k2_cnt(st + 1));
      BAR();
    }
    if constexpr (st + 2 < K2_NST) issue(std::integral_constant<int, st + 2>{}, (st + 2) % K2_NSLOT);
    const char* sb = lds + sl * K2_ST;
#pragma unroll
    for (int u = 0; u < (st == 13 ? 1 : 2); ++u) {
      const int kk = par + 2 * u;
      const bf16x8 af = *(const bf16x8*)(sb + (rb * 4 + kk) * 1024 + lane * 16);
      bf16x8 bfr[5];
#pragma unroll
      for (int c = 0; c < 5; ++c) bfr[c] = *(const bf16x8*)(sb + 16384 + (kk * 5 + c) * 1024 + lane * 16);
#pragma unroll
      for (int c = 0; c < 5; ++c) {
        if constexpr (st < 14) accg[c] = __builtin_amdgcn_mfma_f32_16x16x32_bf16(bfr[c], af, accg[c], 0, 0, 0);
        else accs[c] = __builtin_amdgcn_mfma_f32_16x16x32_bf16(bfr[c], af, accs[c], 0, 0, 0);
      }
    }
  };
  static_for(stage, std::make_integer_sequence<int, K2_NST>{});

  u32x2 xr[3];
#pragma unroll
  for (int c = 0; c < 3; ++c) xr[c] = __builtin_amdgcn_raw_buffer_load_b64(rx, xo + c * 32, 0, 0);

  char* ex = lds + K2_ATT + rb * 10240;
  if (par == 0) {
#pragma unroll
    for (int c = 3; c < 5; ++c) {
      *(f32x4*)(ex + (c * 2 + 0) * 1024 + lane * 16) = accg[c];
      *(f32x4*)(ex + (c * 2 + 1) * 1024 + lane * 16) = accs[c];
    }
  } else {
#pragma unroll
    for (int c = 0; c < 3; ++c) {
      *(f32x4*)(ex + (c * 2 + 0) * 1024 + lane * 16) = accg[c];
      *(f32x4*)(ex + (c * 2 + 1) * 1024 + lane * 16) = accs[c];
    }
  }
  WAITL0();
  BAR();
  const rsrc_t rout = mk_rsrc(out, S_ * D_ * 2);
  auto fin = [&](f32x4 gg, f32x4 ss, int c, int cl) {
    gg += *(const f32x4*)(ex + (c * 2 + 0) * 1024 + lane * 16);
    ss += *(const f32x4*)(ex + (c * 2 + 1) * 1024 + lane * 16);
    float xv[4] = {__uint_as_float(xr[cl][0] << 16), __uint_as_float(xr[cl][0] & 0xffff0000u),
                   __uint_as_float(xr[cl][1] << 16), __uint_as_float(xr[cl][1] & 0xffff0000u)};
    float y[4];
#pragma unroll
    for (int r = 0; r < 4; ++r) y[r] = xv[r] + ss[r] * silu_f(gg[r]);
    u32x2 o = {pack2(y[0], y[1]), pack2(y[2], y[3])};
    __builtin_amdgcn_raw_buffer_store_b64(o, rout, xo + cl * 32, 0, 0);
  };
  if (par == 0) {
    fin(accg[0], accs[0], 0, 0); fin(accg[1], accs[1], 1, 1); fin(accg[2], accs[2], 2, 2);
  } else {
    fin(accg[3], accs[3], 3, 0); fin(accg[4], accs[4], 4, 1);
  }
}

extern "C" void kernel_launch(void* const* d_in, const int* in_sizes, int n_in, void* d_out, int out_size,
                              void* d_ws, size_t ws_size, hipStream_t stream) {
  const u16* x = (const u16*)d_in[0];
  const u16* wqkv = (const u16*)d_in[1];
  const u16* wo = (const u16*)d_in[2];
  const u16* w1 = (const u16*)d_in[4];
  const u16* w2 = (const u16*)d_in[5];
  (void)hipFuncSetAttribute((const void*)k1_kernel, hipFuncAttributeMaxDynamicSharedMemorySize, K1_LDS);
  (void)hipFuncSetAttribute((const void*)k2_kernel, hipFuncAttributeMaxDynamicSharedMemorySize, K2_LDS);
  k1_kernel<<<256, 512, K1_LDS, stream>>>(x, wqkv, w1, w2, wo, (char*)d_ws);
  k2_kernel<<<256, 512, K2_LDS, stream>>>(x, (char*)d_ws, (u16*)d_out);
}
```

```cpp
#include <hip/hip_runtime.h>
#include <hip/hip_bf16.h>
#include <stdint.h>
#include <utility>
#include <type_traits>

typedef unsigned short u16;
typedef __attribute__((ext_vector_type(8))) __bf16 bf16x8;
typedef __attribute__((ext_vector_type(4))) __bf16 bf16x4;
typedef __attribute__((ext_vector_type(2))) __bf16 bf16x2;
typedef __attribute__((ext_vector_type(4))) float f32x4;
typedef __attribute__((ext_vector_type(2))) float f32x2;
typedef __attribute__((ext_vector_type(4))) unsigned u32x4;
typedef __attribute__((ext_vector_type(2))) unsigned u32x2;
#define LAS __attribute__((address_space(3)))
#define GAS __attribute__((address_space(1)))
typedef __amdgpu_buffer_rsrc_t rsrc_t;

#define FENCE() asm volatile("" ::: "memory")
#define BAR() do { asm volatile("s_waitcnt lgkmcnt(0)" ::: "memory"); __builtin_amdgcn_s_barrier(); FENCE(); } while (0)
#define WAITV(n) asm volatile("s_waitcnt vmcnt(%0)" ::"n"(n) : "memory")
#define WAITL0() asm volatile("s_waitcnt lgkmcnt(0)" ::: "memory")

constexpr int S_ = 2048, D_ = 640, F_ = 1728;
constexpr int NKS = 74;
constexpr int NKS_G = 54;
constexpr size_t WS_ACT = 0;
constexpr size_t ACT_BYTES = 128ull * NKS * 1024;
constexpr size_t WS_QKV = WS_ACT + ACT_BYTES;
constexpr size_t QKV_BYTES = 8ull * 128 * 7680;
constexpr size_t WS_WT = WS_QKV + QKV_BYTES + 4096;
constexpr size_t WT_BYTES = 8ull * NKS * 5120;
constexpr size_t WS_CNT = WS_WT + WT_BYTES;
constexpr size_t WS_END = WS_CNT + 32 * 256;

__device__ __forceinline__ rsrc_t mk_rsrc(const void* p, unsigned bytes) {
  return __builtin_amdgcn_make_buffer_rsrc((void*)p, 0, bytes, 0x00020000);
}
__device__ __forceinline__ u32x4 mk_srd(const void* p, unsigned bytes) {
  const unsigned long long a = (unsigned long long)p;
  return (u32x4){(unsigned)a, (unsigned)(a >> 32) & 0xffffu, bytes, 0x00020000u};
}
__device__ __forceinline__ void dma16(u32x4 srd, char* lds, unsigned voff, unsigned soff) {
  const unsigned la = (unsigned)(uintptr_t)(LAS char*)lds;
  asm volatile("s_mov_b32 m0, %0\n\ts_nop 0\n\tbuffer_load_dwordx4 %1, %2, %3 offen lds"
               :: "s"(la), "v"(voff), "s"(srd), "s"(soff));
}
__device__ __forceinline__ bf16x4 tr_read(const char* p) {
  return __builtin_amdgcn_ds_read_tr16_b64_v4bf16((LAS bf16x4*)p);
}
__device__ __forceinline__ bf16x8 cat8(bf16x4 a, bf16x4 b) {
  return __builtin_shufflevector(a, b, 0, 1, 2, 3, 4, 5, 6, 7);
}
__device__ __forceinline__ unsigned pack2(float a, float b) {
  bf16x2 v = {(__bf16)a, (__bf16)b};
  return __builtin_bit_cast(unsigned, v);
}
__device__ __forceinline__ float silu_f(float y) {
  return y * __builtin_amdgcn_rcpf(1.f + __builtin_amdgcn_exp2f(-1.44269504089f * y));
}

#define STAMP(ldsbase, i)
#define STAMP_DUMP(ldsbase, n, dst)
constexpr size_t WS_PROF = 64ull << 20;

template <class Fn, int... Is>
__device__ __forceinline__ void static_for(Fn& f, std::integer_sequence<int, Is...>) {
  (f(std::integral_constant<int, Is>{}), ...);
}

constexpr int MT = 159;
constexpr int NMT = 13, NNT = 19;
constexpr int K1_XB = 160 * 128;
constexpr int K1_WB = 64 * 384;
constexpr int K1_ST = K1_XB + K1_WB;
constexpr int K1_NSLOT = 3;
constexpr int K1_EPI = K1_NSLOT * K1_ST;
constexpr int K1_PROF = K1_EPI + 160 * 8 + 192 * 4;
constexpr int K1_LDS = K1_PROF + 512;

__global__ __launch_bounds__(512) void k1_kernel(const u16* __restrict__ x, const u16* __restrict__ wqkv,
                                                 const u16* __restrict__ w1, const u16* __restrict__ w2,
                                                 const u16* __restrict__ wo, char* __restrict__ ws) {
  extern __shared__ __attribute__((aligned(16))) char lds[];
  const int lane = threadIdx.x & 63;
  const int w = __builtin_amdgcn_readfirstlane(threadIdx.x >> 6);
  const int bid = blockIdx.x;
  const int xcd = bid & 7, idx = bid >> 3;
  int t = xcd * 31 + idx;
  const bool spare = (idx == 31) || (t >= NMT * NNT);
  STAMP(lds + K1_PROF, 0);

  if (bid == 0 && threadIdx.x < 32) {
    *(unsigned*)(ws + WS_CNT + threadIdx.x * 256) = 0u;
  }

  if (spare) {
    const int wk = (idx == 31) ? xcd : 8;
    const u32x4 r2 = mk_srd(w2, F_ * D_ * 2);
    const u32x4 ro = mk_srd(wo, D_ * D_ * 2);
    const rsrc_t rt = mk_rsrc(ws + WS_WT, (unsigned)WT_BYTES);
    unsigned voff[5];
#pragma unroll
    for (int i = 0; i < 5; ++i) {
      const int q = (w * 5 + i) * 64 + lane;
      const int row = q / 80, pc = q % 80;
      const int seg = pc >> 1;
      const int key = (row & 3) | (((row >> 3) & 1) << 2);
      voff[i] = row * 1280 + (((seg ^ key) << 1) | (pc & 1)) * 16;
    }
    const int g = lane >> 4, q4 = (lane & 15) >> 2, p4 = lane & 3;
    const int key = q4 | ((g & 1) << 2);
    const int n_it = (NKS - wk + 8) / 9;
    auto issue = [&](int it) {
      const int ks = wk + it * 9;
      char* dst = lds + (it & 1) * 40960 + w * 5120;
      if (ks < NKS_G) {
#pragma unroll
        for (int i = 0; i < 5; ++i) dma16(r2, dst + i * 1024, voff[i], ks * 40960);
      } else {
#pragma unroll
        for (int i = 0; i < 5; ++i) dma16(ro, dst + i * 1024, voff[i], (ks - NKS_G) * 40960);
      }
    };
    issue(0);
    for (int it = 0; it < n_it; ++it) {
      const int ks = wk + it * 9;
      WAITV(0);
      BAR();
      STAMP(lds + K1_PROF, 1 + it);
      if (it + 1 < n_it) issue(it + 1);
      const char* src = lds + (it & 1) * 40960;
#pragma unroll
      for (int c = 0; c < 5; ++c) {
        const int seg = (w * 5 + c) ^ key;
        const char* a0 = src + (8 * g + q4) * 1280 + seg * 32 + p4 * 8;
        bf16x4 lo = tr_read(a0);
        bf16x4 hi = tr_read(a0 + 4 * 1280);
        bf16x8 f = cat8(lo, hi);
        __builtin_amdgcn_raw_buffer_store_b128(__builtin_bit_cast(u32x4, f), rt, lane * 16 + c * 1024,
                                               (w * NKS + ks) * 5120, 0);
      }
    }
    STAMP(lds + K1_PROF, 12);
    STAMP_DUMP(lds + K1_PROF, 63, ws + WS_PROF + bid * 512);
    return;
  }

  int mt, nt;
  {
    const int band = t / 65;
    const int rem = t - band * 65;
    const int bw = (band == 3) ? 4 : 5;
    mt = rem / bw;
    nt = band * 5 + (rem - mt * bw);
  }
  const int m0 = mt * MT;
  const bool isq = nt < 10;
  const unsigned wstride = isq ? 3840u : 3456u;
  const u32x4 rx = mk_srd(x, S_ * D_ * 2);
  const u32x4 rw = mk_srd(isq ? wqkv : w1, isq ? D_ * 1920 * 2 : D_ * F_ * 2);
  const unsigned wcol0 = (isq ? nt : nt - 10) * 384u;

  if (threadIdx.x < K1_NSLOT * 8) {
    const int sl = threadIdx.x >> 3, pc = threadIdx.x & 7;
    *(u32x4*)(lds + sl * K1_ST + 159 * 128 + pc * 16) = (u32x4){0x3F803F80u, 0x3F803F80u, 0x3F803F80u, 0x3F803F80u};
  }

  const int nx = (w < 4) ? 3 : 2;
  const int xi0 = (w < 4) ? w * 3 : 12 + (w - 4) * 2;
  unsigned xvoff[3], wvoff[3];
#pragma unroll
  for (int i = 0; i < 3; ++i) {
    const int row = (xi0 + i) * 8 + (lane >> 3);
    int grow = m0 + row;
    grow = grow > S_ - 1 ? S_ - 1 : grow;
    xvoff[i] = grow * 1280 + (((lane & 7) ^ ((row >> 1) & 7)) * 16);
    const int q = (w * 3 + i) * 64 + lane;
    const int kr = q / 24, pc = q % 24;
    const int sw = ((kr >> 1) & 1) | (((kr >> 3) & 1) << 1);
    wvoff[i] = kr * wstride + wcol0 + ((((pc >> 1) ^ sw) << 1) | (pc & 1)) * 16;
  }
  const bool last_x_masked = (w == 7);

  auto issue = [&](int st, int sl) {
    char* xb = lds + sl * K1_ST;
    char* wb = xb + K1_XB;
    dma16(rx, xb + (xi0 + 0) * 1024, xvoff[0], st * 128);
    if (last_x_masked) {
      if (lane < 56) dma16(rx, xb + (xi0 + 1) * 1024, xvoff[1], st * 128);
    } else {
      dma16(rx, xb + (xi0 + 1) * 1024, xvoff[1], st * 128);
    }
    if (w < 4) dma16(rx, xb + (xi0 + 2) * 1024, xvoff[2], st * 128);
#pragma unroll
    for (int i = 0; i < 3; ++i) dma16(rw, wb + (w * 3 + i) * 1024, wvoff[i], st * 64 * wstride);
  };

  issue(0, 0);
  issue(1, 1);

  const int mi = w & 1, ni = w >> 1;
  const int m = lane & 15, g = lane >> 4;
  const int q4 = (lane & 15) >> 2, p4 = lane & 3;
  const int xrd0 = (mi * 80 + m) * 128 + ((g ^ (m >> 1)) * 16);
  int wrd[3];
  {
    const int sw = ((q4 >> 1) & 1) | ((g & 1) << 1);
#pragma unroll
    for (int c = 0; c < 3; ++c) wrd[c] = K1_XB + (8 * g + q4) * 384 + (((ni * 3 + c) ^ sw) * 32) + p4 * 8;
  }

  f32x4 acc[5][3];
#pragma unroll
  for (int a = 0; a < 5; ++a)
#pragma unroll
    for (int c = 0; c < 3; ++c) acc[a][c] = (f32x4){0.f, 0.f, 0.f, 0.f};
  float ssum[3] = {0.f, 0.f, 0.f}, ssq[3] = {0.f, 0.f, 0.f};
  const bf16x2 ones2 = {(__bf16)1.0f, (__bf16)1.0f};

  int sl = 0;
  for (int st = 0; st < 10; ++st) {
    if (st < 9) {
      if (w < 4) WAITV(6); else WAITV(5);
    } else {
      WAITV(0);
    }
    BAR();
    STAMP(lds + K1_PROF, 1 + st);
    if (st + 2 < 10) {
      int s2 = sl + 2; if (s2 >= K1_NSLOT) s2 -= K1_NSLOT;
      issue(st + 2, s2);
    }
    const char* sb = lds + sl * K1_ST;
#pragma unroll
    for (int i = 0; i < 3; ++i) {
      if (i < nx) {
        const u32x4 v = *(const u32x4*)(sb + (xi0 + i) * 1024 + lane * 16);
#pragma unroll
        for (int j = 0; j < 4; ++j) {
          const unsigned uj = v[j];
          const bf16x2 p = __builtin_bit_cast(bf16x2, uj);
          ssum[i] = __builtin_amdgcn_fdot2_f32_bf16(p, ones2, ssum[i], false);
          ssq[i] = __builtin_amdgcn_fdot2_f32_bf16(p, p, ssq[i], false);
        }
      }
    }
#pragma unroll
    for (int ks = 0; ks < 2; ++ks) {
      bf16x8 xf[5], wf[3];
#pragma unroll
      for (int a = 0; a < 5; ++a) xf[a] = *(const bf16x8*)(sb + ((xrd0 ^ (ks * 64)) + a * 2048));
#pragma unroll
      for (int c = 0; c < 3; ++c) {
        const char* p = sb + wrd[c] + ks * 32 * 384;
        wf[c] = cat8(tr_read(p), tr_read(p + 4 * 384));
      }
#pragma unroll
      for (int a = 0; a < 5; ++a)
#pragma unroll
        for (int c = 0; c < 3; ++c)
          acc[a][c] = __builtin_amdgcn_mfma_f32_16x16x32_bf16(wf[c], xf[a], acc[a][c], 0, 0, 0);
    }
    sl = (sl + 1 == K1_NSLOT) ? 0 : sl + 1;
  }

  STAMP(lds + K1_PROF, 11);
  float* e_stat = (float*)(lds + K1_EPI);
  float* e_cs = (float*)(lds + K1_EPI + 160 * 8);
#pragma unroll
  for (int i = 0; i < 3; ++i) {
    if (i < nx) {
      float s = ssum[i], q = ssq[i];
#pragma unroll
      for (int o = 1; o < 8; o <<= 1) { s += __shfl_xor(s, o); q += __shfl_xor(q, o); }
      const float mean = s * (1.f / D_);
      float var = q * (1.f / D_) - mean * mean;
      var = var > 0.f ? var : 0.f;
      const float rstd = __builtin_amdgcn_rsqf(var + 1e-5f);
      if ((lane & 7) == 0) {
        const int row = (xi0 + i) * 8 + (lane >> 3);
        *(f32x2*)(e_stat + row * 2) = (f32x2){rstd, -mean * rstd};
      }
    }
  }
  if (mi == 1 && m == 15) {
#pragma unroll
    for (int c = 0; c < 3; ++c) *(f32x4*)(e_cs + (ni * 3 + c) * 16 + g * 4) = acc[4][c];
  }
  WAITL0();
  BAR();

  f32x4 cs[3];
#pragma unroll
  for (int c = 0; c < 3; ++c) cs[c] = *(const f32x4*)(e_cs + (ni * 3 + c) * 16 + g * 4);

  const rsrc_t ro = isq ? mk_rsrc(ws + WS_QKV, (unsigned)QKV_BYTES) : mk_rsrc(ws + WS_ACT, (unsigned)ACT_BYTES);
  unsigned coff[3];
#pragma unroll
  for (int c = 0; c < 3; ++c) {
    const int col = (isq ? nt : nt - 10) * 192 + (ni * 3 + c) * 16 + g * 4;
    if (isq) {
      const int sec = col / 640, cc = col - sec * 640;
      const int hg = cc / 80, pc = sec * 20 + (cc - hg * 80) / 4;
      coff[c] = hg * (128 * 7680) + pc * 128;
    } else {
      coff[c] = (col >> 5) * 1024 + ((col >> 3) & 3) * 256 + ((col >> 2) & 1) * 8;
    }
  }
#pragma unroll
  for (int a = 0; a < 5; ++a) {
    const int lrow = mi * 80 + a * 16 + m;
    const int grow = m0 + lrow;
    const f32x2 stt = *(const f32x2*)(e_stat + lrow * 2);
    const bool ok = (lrow < MT) && (grow < S_);
    const unsigned roff = isq ? (grow >> 4) * 7680 + (grow & 15) * 8 : (grow >> 4) * (NKS * 1024) + (grow & 15) * 16;
#pragma unroll
    for (int c = 0; c < 3; ++c) {
      f32x4 y;
#pragma unroll
      for (int r = 0; r < 4; ++r) y[r] = acc[a][c][r] * stt[0] + cs[c][r] * stt[1];
      if (!isq) {
#pragma unroll
        for (int r = 0; r < 4; ++r) y[r] = silu_f(y[r]);
      }
      u32x2 o = {pack2(y[0], y[1]), pack2(y[2], y[3])};
      if (ok) __builtin_amdgcn_raw_buffer_store_b64(o, ro, roff + coff[c], 0, 0);
    }
  }
  STAMP(lds + K1_PROF, 12);
  STAMP_DUMP(lds + K1_PROF, 63, ws + WS_PROF + bid * 512);
}

constexpr int K2_ST = 36 * 1024;
constexpr int K2_NSLOT = 3;
constexpr int K2_ATT = K2_NSLOT * K2_ST;
constexpr int K2_PROF = K2_ATT + 40 * 1024;
constexpr int K2_LDS = K2_PROF + 512;
constexpr int K2_NST = 19;

__device__ __forceinline__ constexpr int k2_ks0(int st) { return st < 14 ? st * 4 : NKS_G + (st - 14) * 4; }
__device__ __forceinline__ constexpr int k2_cnt(int st) { return st >= K2_NST ? 0 : (st == 13 ? 3 : 5); }

__global__ __launch_bounds__(512) void k2_kernel(const u16* __restrict__ x, char* __restrict__ ws,
                                                 u16* __restrict__ out) {
  extern __shared__ __attribute__((aligned(16))) char lds[];
  const int lane = threadIdx.x & 63;
  const int w = __builtin_amdgcn_readfirstlane(threadIdx.x >> 6);
  const int bid = blockIdx.x;
  const int xcd = bid & 7, idx = bid >> 3;
  const int mt = xcd * 4 + (idx >> 3), nt = idx & 7;
  const int rb = w & 3, par = w >> 2;
  STAMP(lds + K2_PROF, 0);
  const int m = lane & 15, g = lane >> 4;
  const int q4 = (lane & 15) >> 2, p4 = lane & 3;

  const rsrc_t ract = mk_rsrc(ws + WS_ACT, (unsigned)ACT_BYTES);
  const u32x4 sact = mk_srd(ws + WS_ACT, (unsigned)ACT_BYTES);
  const u32x4 rqkv = mk_srd(ws + WS_QKV, (unsigned)QKV_BYTES);
  const u32x4 rwt = mk_srd(ws + WS_WT, (unsigned)WT_BYTES);
  GAS unsigned* cnt = (GAS unsigned*)(ws + WS_CNT + mt * 256);

  {
    char* img = lds + K2_ATT;
    const unsigned base = (nt * 128 + 4 * mt) * 7680;
    const unsigned hbase = (mt == 0) ? base : base - 7680;
    if (w < 7 || lane < 32) dma16(rqkv, img + w * 1024, lane * 16, hbase + w * 1024);
#pragma unroll
    for (int i = 0; i < 4; ++i) {
      const int j = i * 8 + w;
      if (j < 30) dma16(rqkv, img + 7680 + j * 1024, lane * 16, base + j * 1024);
    }
  }
  const rsrc_t rx = mk_rsrc(x, S_ * D_ * 2);
  const unsigned xo = (mt * 64 + rb * 16 + m) * 1280 + (nt * 80 + par * 48 + g * 4) * 2;

  auto issue = [&](auto stc, int sl) {
    constexpr int st = decltype(stc)::value;
    constexpr int ks0 = k2_ks0(st);
    char* sb = lds + sl * K2_ST;
    const unsigned abase = ((mt * 4 + rb) * NKS + ks0 + par) * 1024;
    dma16(sact, sb + (rb * 4 + par) * 1024, lane * 16, abase);
    if (st != 13) dma16(sact, sb + (rb * 4 + par + 2) * 1024, lane * 16, abase + 2048);
    const unsigned bbase = (nt * NKS + ks0) * 5120;
    char* bb = sb + 16384;
    if (st != 13) {
      dma16(rwt, bb + w * 2560, lane * 16, bbase + w * 2560);
      dma16(rwt, bb + w * 2560 + 1024, lane * 16, bbase + w * 2560 + 1024);
      if (lane < 32) dma16(rwt, bb + w * 2560 + 2048, lane * 16, bbase + w * 2560 + 2048);
    } else {
      dma16(rwt, bb + w * 1280, lane * 16, bbase + w * 1280);
      if (lane < 16) dma16(rwt, bb + w * 1280 + 1024, lane * 16, bbase + w * 1280 + 1024);
    }
  };
  issue(std::integral_constant<int, 0>{}, 0);
  issue(std::integral_constant<int, 1>{}, 1);

  WAITV(10);
  BAR();
  STAMP(lds + K2_PROF, 1);
  {
    const char* img = lds + K2_ATT;
    const int hh = w & 3, b = w >> 2;
    const float sc = 0.22360679775f * 1.44269504089f;
#pragma unroll
    for (int p = 0; p < 2; ++p) {
      const int lq = 16 + 32 * b + 2 * m + p;
      int lp = lq - 32; lp = lp < 0 ? 0 : lp;
      const char* aq = img + (lq >> 4) * 7680 + (lq & 15) * 8 + hh * 640;
      const char* ap = img + (lp >> 4) * 7680 + (lp & 15) * 8 + hh * 640;
      bf16x4 q0 = *(const bf16x4*)(aq + g * 128);
      bf16x4 q1 = *(const bf16x4*)(aq + 4 * 128);
      u32x2 q1u = __builtin_bit_cast(u32x2, q1);
      q1u[0] = g == 0 ? q1u[0] : 0u; q1u[1] = g == 0 ? q1u[1] : 0u;
      const bf16x8 qf = cat8(q0, __builtin_bit_cast(bf16x4, q1u));
      const bf16x8 kc = cat8(*(const bf16x4*)(aq + 2560 + g * 128), *(const bf16x4*)(aq + 2560 + 512));
      const bf16x8 kp = cat8(*(const bf16x4*)(ap + 2560 + g * 128), *(const bf16x4*)(ap + 2560 + 512));
      const f32x4 z4 = {0.f, 0.f, 0.f, 0.f};
      f32x4 sp = __builtin_amdgcn_mfma_f32_16x16x32_bf16(kp, qf, z4, 0, 0, 0);
      f32x4 scur = __builtin_amdgcn_mfma_f32_16x16x32_bf16(kc, qf, z4, 0, 0, 0);
      const int kq = 16 + 32 * b + 2 * (4 * g + q4) + p;
      int kpv = kq - 32; kpv = kpv < 0 ? 0 : kpv;
      const char* vq = img + (kq >> 4) * 7680 + (kq & 15) * 8 + hh * 640 + 5120;
      const char* vp = img + (kpv >> 4) * 7680 + (kpv & 15) * 8 + hh * 640 + 5120;
      const bf16x8 v0 = cat8(tr_read(vp + p4 * 128), tr_read(vq + p4 * 128));
      const bf16x8 v1 = cat8(tr_read(vp + 512), tr_read(vq + 512));
      const bool prev_ok = !(mt == 0 && b == 0);
      float e[8];
      float mx = -3.0e38f;
#pragma unroll
      for (int r = 0; r < 4; ++r) {
        const int jj = 4 * g + r;
        const bool okp = prev_ok && (jj >= m + 9);
        const bool okc = (jj <= m) && (m - jj <= 7);
        e[r] = okp ? sp[r] * sc : -3.0e38f;
        e[4 + r] = okc ? scur[r] * sc : -3.0e38f;
        mx = fmaxf(mx, fmaxf(e[r], e[4 + r]));
      }
      mx = fmaxf(mx, __shfl_xor(mx, 16));
      mx = fmaxf(mx, __shfl_xor(mx, 32));
      float sum = 0.f;
#pragma unroll
      for (int r = 0; r < 8; ++r) { e[r] = __builtin_amdgcn_exp2f(e[r] - mx); sum += e[r]; }
      sum += __shfl_xor(sum, 16);
      sum += __shfl_xor(sum, 32);
      const float inv = __builtin_amdgcn_rcpf(sum);
      u32x4 pu = {pack2(e[0] * inv, e[1] * inv), pack2(e[2] * inv, e[3] * inv), pack2(e[4] * inv, e[5] * inv),
                  pack2(e[6] * inv, e[7] * inv)};
      const bf16x8 pf = __builtin_bit_cast(bf16x8, pu);
      const f32x4 o0 = __builtin_amdgcn_mfma_f32_16x16x32_bf16(v0, pf, z4, 0, 0, 0);
      const f32x4 o1 = __builtin_amdgcn_mfma_f32_16x16x32_bf16(v1, pf, z4, 0, 0, 0);
      const int R = mt * 64 + 32 * b + 2 * m + p;
      const unsigned rowoff = (R >> 4) * (NKS * 1024) + (R & 15) * 16 + NKS_G * 1024;
      const int P0 = 20 * nt + 5 * hh + g, P1 = 20 * nt + 5 * hh + 4;
      const unsigned a0 = rowoff + (P0 >> 3) * 1024 + ((P0 >> 1) & 3) * 256 + (P0 & 1) * 8;
      const unsigned a1 = rowoff + (P1 >> 3) * 1024 + ((P1 >> 1) & 3) * 256 + (P1 & 1) * 8;
      u32x2 s0 = {pack2(o0[0], o0[1]), pack2(o0[2], o0[3])};
      u32x2 s1 = {pack2(o1[0], o1[1]), pack2(o1[2], o1[3])};
      __builtin_amdgcn_raw_buffer_store_b64(s0, ract, a0, 0, 16);
      if (g == 0) __builtin_amdgcn_raw_buffer_store_b64(s1, ract, a1, 0, 16);
    }
  }
  STAMP(lds + K2_PROF, 2);
  WAITV(0);
  BAR();
  if (threadIdx.x == 0) __hip_atomic_fetch_add(cnt, 1u, __ATOMIC_RELAXED, __HIP_MEMORY_SCOPE_AGENT);

  f32x4 accg[5], accs[5];
#pragma unroll
  for (int c = 0; c < 5; ++c) { accg[c] = (f32x4){0.f, 0.f, 0.f, 0.f}; accs[c] = (f32x4){0.f, 0.f, 0.f, 0.f}; }

  auto stage = [&](auto stc) {
    constexpr int st = decltype(stc)::value;
    constexpr int sl = st % K2_NSLOT;
    if constexpr (st == 12) {
      if (w == 0) {
        unsigned spins = 0;
        while (__builtin_amdgcn_readfirstlane(__hip_atomic_load(cnt, __ATOMIC_RELAXED, __HIP_MEMORY_SCOPE_AGENT)) < 8u) {
          __builtin_amdgcn_s_sleep(1);
          if (++spins > (1u << 22)) break;
        }
        __builtin_amdgcn_fence(__ATOMIC_ACQUIRE, "agent");
        WAITV(0);
      }
    }
    if constexpr (st > 0) {
      WAITV(k2_cnt(st + 1));
      BAR();
    }
    STAMP(lds + K2_PROF, 3 + st);
    if constexpr (st + 2 < K2_NST) issue(std::integral_constant<int, st + 2>{}, (st + 2) % K2_NSLOT);
    const char* sb = lds + sl * K2_ST;
#pragma unroll
    for (int u = 0; u < (st == 13 ? 1 : 2); ++u) {
      const int kk = par + 2 * u;
      const bf16x8 af = *(const bf16x8*)(sb + (rb * 4 + kk) * 1024 + lane * 16);
      bf16x8 bfr[5];
#pragma unroll
      for (int c = 0; c < 5; ++c) bfr[c] = *(const bf16x8*)(sb + 16384 + (kk * 5 + c) * 1024 + lane * 16);
#pragma unroll
      for (int c = 0; c < 5; ++c) {
        if constexpr (st < 14) accg[c] = __builtin_amdgcn_mfma_f32_16x16x32_bf16(bfr[c], af, accg[c], 0, 0, 0);
        else accs[c] = __builtin_amdgcn_mfma_f32_16x16x32_bf16(bfr[c], af, accs[c], 0, 0, 0);
      }
    }
  };
  static_for(stage, std::make_integer_sequence<int, K2_NST>{});

  STAMP(lds + K2_PROF, 22);
  u32x2 xr[3];
#pragma unroll
  for (int c = 0; c < 3; ++c) xr[c] = __builtin_amdgcn_raw_buffer_load_b64(rx, xo + c * 32, 0, 0);

  char* ex = lds + K2_ATT + rb * 10240;
  if (par == 0) {
#pragma unroll
    for (int c = 3; c < 5; ++c) {
      *(f32x4*)(ex + (c * 2 + 0) * 1024 + lane * 16) = accg[c];
      *(f32x4*)(ex + (c * 2 + 1) * 1024 + lane * 16) = accs[c];
    }
  } else {
#pragma unroll
    for (int c = 0; c < 3; ++c) {
      *(f32x4*)(ex + (c * 2 + 0) * 1024 + lane * 16) = accg[c];
      *(f32x4*)(ex + (c * 2 + 1) * 1024 + lane * 16) = accs[c];
    }
  }
  WAITL0();
  BAR();
  const rsrc_t rout = mk_rsrc(out, S_ * D_ * 2);
  auto fin = [&](f32x4 gg, f32x4 ss, int c, int cl) {
    gg += *(const f32x4*)(ex + (c * 2 + 0) * 1024 + lane * 16);
    ss += *(const f32x4*)(ex + (c * 2 + 1) * 1024 + lane * 16);
    float xv[4] = {__uint_as_float(xr[cl][0] << 16), __uint_as_float(xr[cl][0] & 0xffff0000u),
                   __uint_as_float(xr[cl][1] << 16), __uint_as_float(xr[cl][1] & 0xffff0000u)};
    float y[4];
#pragma unroll
    for (int r = 0; r < 4; ++r) y[r] = xv[r] + ss[r] * silu_f(gg[r]);
    u32x2 o = {pack2(y[0], y[1]), pack2(y[2], y[3])};
    __builtin_amdgcn_raw_buffer_store_b64(o, rout, xo + cl * 32, 0, 0);
  };
  if (par == 0) {
    fin(accg[0], accs[0], 0, 0); fin(accg[1], accs[1], 1, 1); fin(accg[2], accs[2], 2, 2);
  } else {
    fin(accg[3], accs[3], 3, 0); fin(accg[4], accs[4], 4, 1);
  }
  STAMP(lds + K2_PROF, 23);
  STAMP_DUMP(lds + K2_PROF, 63, ws + WS_PROF + (256 + bid) * 512);
}

extern "C" void kernel_launch(void* const* d_in, const int* in_sizes, int n_in, void* d_out, int out_size,
                              void* d_ws, size_t ws_size, hipStream_t stream) {
  const u16* x = (const u16*)d_in[0];
  const u16* wqkv = (const u16*)d_in[1];
  const u16* wo = (const u16*)d_in[2];
  const u16* w1 = (const u16*)d_in[4];
  const u16* w2 = (const u16*)d_in[5];
  (void)hipFuncSetAttribute((const void*)k1_kernel, hipFuncAttributeMaxDynamicSharedMemorySize, K1_LDS);
  (void)hipFuncSetAttribute((const void*)k2_kernel, hipFuncAttributeMaxDynamicSharedMemorySize, K2_LDS);
  k1_kernel<<<256, 512, K1_LDS, stream>>>(x, wqkv, w1, w2, wo, (char*)d_ws);
  k2_kernel<<<256, 512, K2_LDS, stream>>>(x, (char*)d_ws, (u16*)d_out);
}
```

```cpp
#include <hip/hip_runtime.h>
#include <hip/hip_bf16.h>
#include <stdint.h>
#include <utility>
#include <type_traits>

typedef unsigned short u16;
typedef unsigned long long u64;
typedef __attribute__((ext_vector_type(8))) __bf16 bf16x8;
typedef __attribute__((ext_vector_type(4))) __bf16 bf16x4;
typedef __attribute__((ext_vector_type(2))) __bf16 bf16x2;
typedef __attribute__((ext_vector_type(4))) float f32x4;
typedef __attribute__((ext_vector_type(2))) float f32x2;
typedef __attribute__((ext_vector_type(16))) float f32x16;
typedef __attribute__((ext_vector_type(4))) unsigned u32x4;
typedef __attribute__((ext_vector_type(2))) unsigned u32x2;
#define LAS __attribute__((address_space(3)))
typedef __amdgpu_buffer_rsrc_t rsrc_t;
template <int N> using IC = std::integral_constant<int, N>;

#define FENCE() asm volatile("" ::: "memory")
#define BAR() do { asm volatile("s_waitcnt lgkmcnt(0)" ::: "memory"); __builtin_amdgcn_s_barrier(); FENCE(); } while (0)
#define WAITV(n) asm volatile("s_waitcnt vmcnt(%0)" ::"n"(n) : "memory")
#define SCHED() __builtin_amdgcn_sched_barrier(0)
#define PIN(x) asm volatile("" : "+v"(x))

constexpr unsigned SPIN_CAP = 1u << 20;
constexpr unsigned CODE_BYTES = 43520;
constexpr int S_ = 2048, D_ = 640, F_ = 1728;
constexpr int NKS = 74, NKS_G = 54;
constexpr int LFD1 = 2, LFD2 = 2;
constexpr unsigned WS_ACT = 0;
constexpr unsigned WS_QKV = WS_ACT + 128u * NKS * 1024;
constexpr unsigned WS_WT = WS_QKV + 128u * 60 * 1024;
constexpr unsigned WS_STAT = WS_WT + 8u * NKS * 5120;
constexpr unsigned WS_CS = WS_STAT + 2048u * 16;
constexpr unsigned WS_E1 = WS_CS + 4u * 3648 * 8;
constexpr unsigned WS_E2 = WS_E1 + 32768;
constexpr unsigned WS_E3 = WS_E2 + 32768;
constexpr unsigned WS_E4 = WS_E3 + 32768;
constexpr unsigned WS_E5 = WS_E4 + 32768;
constexpr unsigned WS_M = WS_E5 + 32768;
constexpr unsigned WS_END = WS_M + 256;
constexpr unsigned OOB = 0x7ffffff0u;

__device__ __forceinline__ constexpr int WPART(int i) { return (i & 3) * 16384; }
__device__ __forceinline__ constexpr int BPART(int i) { return ((i + 2) & 3) * 20480; }
constexpr int L_EX = 0;
__device__ __forceinline__ constexpr int APART(int i) { return 81920 + (i & 3) * 16384; }
constexpr int L_CU = 65536;
constexpr int L_MISC = 147456;
constexpr int L_QIMG = L_MISC;
constexpr int L_CS = L_MISC + 10240;
constexpr int L_ST = L_CS + 1024;
constexpr int L_SX = L_MISC + 15872;
constexpr int L_TS = L_MISC + 16128;
constexpr int L_TOTAL = 163840;

__device__ __forceinline__ rsrc_t mk_rsrc(const void* p, unsigned bytes) {
  return __builtin_amdgcn_make_buffer_rsrc((void*)p, 0, bytes, 0x00020000);
}
__device__ __forceinline__ u32x4 mk_srd(const void* p, unsigned bytes) {
  const u64 a = (u64)p;
  return (u32x4){(unsigned)a, (unsigned)(a >> 32) & 0xffffu, bytes, 0x00020000u};
}
__device__ __forceinline__ void dma16(u32x4 srd, unsigned la, unsigned voff, unsigned soff) {
  asm volatile("s_mov_b32 m0, %0\n\ts_nop 0\n\tbuffer_load_dwordx4 %1, %2, %3 offen lds"
               ::"s"(la), "v"(voff), "s"(srd), "s"(soff) : "memory");
}
template <int LI, int SI, bool SC1>
__device__ __forceinline__ void dma1(u32x4 srd, unsigned lbase, unsigned voff, unsigned sbase) {
  unsigned tmp;
  if constexpr (SC1)
    asm volatile("s_add_u32 m0, %1, %5\n\ts_add_u32 %0, %4, %6\n\tbuffer_load_dwordx4 %2, %3, %0 offen sc1 lds"
                 : "=&s"(tmp) : "s"(lbase), "v"(voff), "s"(srd), "s"(sbase), "n"(LI), "n"(SI) : "memory", "scc");
  else
    asm volatile("s_add_u32 m0, %1, %5\n\ts_add_u32 %0, %4, %6\n\tbuffer_load_dwordx4 %2, %3, %0 offen lds"
                 : "=&s"(tmp) : "s"(lbase), "v"(voff), "s"(srd), "s"(sbase), "n"(LI), "n"(SI) : "memory", "scc");
}
template <int N, int LI, int SI>
__device__ __forceinline__ void dma_run(u32x4 srd, unsigned lbase, unsigned voff, unsigned sbase) {
  unsigned tmp;
#define DR_HEAD "s_add_u32 m0, %1, %5\n\ts_add_u32 %0, %4, %6\n\tbuffer_load_dwordx4 %2, %3, %0 offen sc1 lds"
#define DR_MORE(o) "\n\tbuffer_load_dwordx4 %2, %3, %0 offen offset:" #o " sc1 lds"
#define DR_OPS : "=&s"(tmp) : "s"(lbase), "v"(voff), "s"(srd), "s"(sbase), "n"(LI), "n"(SI) : "memory", "scc"
  if constexpr (N == 1) asm volatile(DR_HEAD DR_OPS);
  else if constexpr (N == 2) asm volatile(DR_HEAD DR_MORE(1024) DR_OPS);
  else if constexpr (N == 3) asm volatile(DR_HEAD DR_MORE(1024) DR_MORE(2048) DR_OPS);
  else asm volatile(DR_HEAD DR_MORE(1024) DR_MORE(2048) DR_MORE(3072) DR_OPS);
}
#define PINS(x) asm volatile("" : "+s"(x))
__device__ __forceinline__ void ld16(u32x4& d, u32x4 srd, unsigned voff, unsigned soff) {
  asm volatile("buffer_load_dwordx4 %0, %1, %2, %3 offen" : "=v"(d) : "v"(voff), "s"(srd), "s"(soff) : "memory");
}
__device__ __forceinline__ void ld16c(u32x4& d, u32x4 srd, unsigned voff, unsigned soff) {
  asm volatile("buffer_load_dwordx4 %0, %1, %2, %3 offen sc1" : "=v"(d) : "v"(voff), "s"(srd), "s"(soff) : "memory");
}
__device__ __forceinline__ void ld8c(u32x2& d, u32x4 srd, unsigned voff, unsigned soff) {
  asm volatile("buffer_load_dwordx2 %0, %1, %2, %3 offen sc1" : "=v"(d) : "v"(voff), "s"(srd), "s"(soff) : "memory");
}
__device__ __forceinline__ void ld8(u32x2& d, u32x4 srd, unsigned voff, unsigned soff) {
  asm volatile("buffer_load_dwordx2 %0, %1, %2, %3 offen" : "=v"(d) : "v"(voff), "s"(srd), "s"(soff) : "memory");
}
template <class T> __device__ __forceinline__ T ldsr(unsigned a) { return *(const LAS T*)(uintptr_t)a; }
template <class T> __device__ __forceinline__ void ldsw(unsigned a, T v) { *(LAS T*)(uintptr_t)a = v; }
__device__ __forceinline__ bf16x4 tr_read(unsigned a) {
  return __builtin_amdgcn_ds_read_tr16_b64_v4bf16((LAS bf16x4*)(uintptr_t)a);
}
__device__ __forceinline__ bf16x8 cat8(bf16x4 a, bf16x4 b) {
  return __builtin_shufflevector(a, b, 0, 1, 2, 3, 4, 5, 6, 7);
}
__device__ __forceinline__ unsigned pack2(float a, float b) {
  bf16x2 v = {(__bf16)a, (__bf16)b};
  return __builtin_bit_cast(unsigned, v);
}
__device__ __forceinline__ float silu_f(float y) {
  return y * __builtin_amdgcn_rcpf(1.f + __builtin_amdgcn_exp2f(-1.44269504089f * y));
}
__device__ __forceinline__ float bflo(unsigned u) { return __uint_as_float(u << 16); }
__device__ __forceinline__ float bfhi(unsigned u) { return __uint_as_float(u & 0xffff0000u); }
#define MFMA(a, b, c) __builtin_amdgcn_mfma_f32_16x16x32_bf16((a), (b), (c), 0, 0, 0)
#define MFMA32(a, b, c) __builtin_amdgcn_mfma_f32_32x32x16_bf16((a), (b), (c), 0, 0, 0)
template <int CTRL> __device__ __forceinline__ float dppx(float v) {
  return __builtin_bit_cast(float, __builtin_amdgcn_update_dpp(0, __builtin_bit_cast(int, v), CTRL, 0xf, 0xf, true));
}
struct F2 { float a, b; };
__device__ __forceinline__ F2 sw16(float v) {
  const unsigned u = __float_as_uint(v);
  auto r = __builtin_amdgcn_permlane16_swap(u, u, false, false);
  return {__uint_as_float(r[0]), __uint_as_float(r[1])};
}
__device__ __forceinline__ F2 sw32(float v) {
  const unsigned u = __float_as_uint(v);
  auto r = __builtin_amdgcn_permlane32_swap(u, u, false, false);
  return {__uint_as_float(r[0]), __uint_as_float(r[1])};
}
__device__ __forceinline__ float sum_g(float v) { F2 a = sw16(v); v = a.a + a.b; F2 b = sw32(v); return b.a + b.b; }
__device__ __forceinline__ float max_g(float v) { F2 a = sw16(v); v = fmaxf(a.a, a.b); F2 b = sw32(v); return fmaxf(b.a, b.b); }
__device__ __forceinline__ float wave_sum(float s) {
  s += dppx<0xB1>(s); s += dppx<0x4E>(s); s += dppx<0x141>(s); s += dppx<0x140>(s);
  return sum_g(s);
}


template <class Fn, int... Is>
__device__ __forceinline__ void static_for_impl(Fn& f, std::integer_sequence<int, Is...>) { (f(IC<Is>{}), ...); }
template <int N, class Fn>
__device__ __forceinline__ void static_for(Fn&& f) { static_for_impl(f, std::make_integer_sequence<int, N>{}); }

constexpr int lf_order(int t, int c, int G, int D, int n) {
  int L = t + ((c - t) % G + G) % G;
  if (L >= n) return t;
  const int d = L < D ? L : D;
  if (t == L - d) return L;
  if (t > L - d) return t - 1;
  return t;
}

constexpr u64 lf_table(int c, int G, int D, int n) {
  u64 v = 0;
  for (int t = 0; t < n; ++t) v |= (u64)lf_order(t, c, G, D, n) << (4 * t);
  return v;
}
template <int G, int D, int N, int HI> __device__ __forceinline__ unsigned lf_pick(int c) {
  unsigned v = (unsigned)(lf_table(0, G, D, N) >> (32 * HI));
  static_for<G - 1>([&](auto kc) {
    constexpr int k = decltype(kc)::value + 1;
    constexpr unsigned tv = (unsigned)(lf_table(k, G, D, N) >> (32 * HI));
    if (c == k) v = tv;
  });
  v = __builtin_amdgcn_readfirstlane(v);
  PINS(v);
  return v;
}

__device__ __forceinline__ int nib(unsigned word, unsigned idx) {
  unsigned r;
  asm("s_bfe_u32 %0, %1, %2" : "=s"(r) : "s"(word), "s"((4u << 16) | (4u * idx)) : "scc");
  return (int)r;
}

enum { LB_B = 100, LB_IMG = 300 };
struct Op { int step, label, cnt; };
constexpr Op kL[] = {
    {-1, 0, 16}, {-1, 1, 16}, {0, 2, 16}, {1, 3, 16}, {2, 4, 16}, {3, 5, 16}, {3, 6, 16}, {4, 7, 16}, {5, 8, 16}, {6, 9, 16},
    {7, 10, 8}, {8, 11, 8}, {9, 12, 8}, {10, 13, 8}, {11, 14, 8}, {12, 15, 8}, {13, 16, 8},
    {14, 17, 8}, {15, 18, 8}, {15, LB_B + 0, 10}, {16, 19, 8}, {16, LB_B + 1, 10},
    {18, LB_IMG, 20},
};
constexpr int wait_L(int T) {
  constexpr int N = sizeof(kL) / sizeof(Op);
  if (T >= 20) return 63;
  int last = -1;
  for (int i = 0; i < N; ++i) if (kL[i].label == T) last = i;
  int n = 0;
  for (int i = last + 1; i < N; ++i) if (kL[i].step < T) n += kL[i].cnt;
  return n;
}
constexpr int T_E5 = 10, T_E1 = 14, T_E2 = 20, T_C5 = 15, T_C1 = 18;
constexpr int NP = 19, P_E3 = 2, P_C3 = 5, P_E4 = 8, P_C4 = 11;

__global__ __launch_bounds__(768, 3) void fused_kernel(const u16* __restrict__ x, const u16* __restrict__ wqkv,
                                                       const u16* __restrict__ w1, const u16* __restrict__ w2,
                                                       const u16* __restrict__ wo, char* __restrict__ ws,
                                                       u16* __restrict__ out) {
  extern __shared__ __attribute__((aligned(16))) char lds[];
  const unsigned lb = (unsigned)(uintptr_t)(LAS char*)lds;
  {
    u64 pc;
    asm volatile("s_getpc_b64 %0" : "=s"(pc));
    const u32x4 sc = mk_srd((const void*)pc, CODE_BYTES);
    asm volatile("s_mov_b32 m0, %0\n\ts_nop 0\n\tbuffer_load_dword %1, %2, 0 offen lds"
                 ::"s"(lb + L_TS), "v"((threadIdx.x * 8 + ((blockIdx.x >> 3) & 7)) * 128u), "s"(sc) : "memory");
  }
  const int lane = threadIdx.x & 63;
  const int w = __builtin_amdgcn_readfirstlane(threadIdx.x >> 6);
  const int bid = blockIdx.x;
  const int xcd = bid & 7, idx = bid >> 3;
  const int m = lane & 15, g = lane >> 4, q4 = (lane & 15) >> 2, p4 = lane & 3;
  const unsigned l16 = lane * 16;
  const u32x4 sx = mk_srd(x, S_ * D_ * 2);
  const u32x4 sw = mk_srd(ws, WS_END);
  const rsrc_t rw = mk_rsrc(ws, WS_END);

  const bool spare = idx >= 30;
  const int t1 = spare ? 0 : xcd * 30 + idx;
  const int band = t1 / 80, rem = t1 - band * 80;
  const int mt1 = rem / 5;
  const int jj = band * 5 + (rem - mt1 * 5);
  const int wk = (idx - 30) * 8 + xcd;
  const bool hasB = !spare && jj < 12;
  const int chA = jj, chB = jj < 12 ? 15 + jj : jj;
  const int mt2 = xcd * 4 + (idx >> 3), nt = idx & 7;
  const int R0 = mt2 * 64 + (nt & 1) * 32, ho = nt >> 1;
  const int cu_j = band * 5 + (rem >> 4), cu_t = (rem >> 2) & 3;
  const int cu_ck = cu_t < 2 ? 2 * cu_j + cu_t : cu_t == 2 ? 30 + cu_j : 45 + cu_j;
  const bool hasCU = !spare && cu_ck < 57;

  u64 tag;
  auto load_tag = [&]() {
    u32x2 t;
    ld8c(t, sw, WS_M, 0);
    WAITV(0);
    PIN(t);
    tag = (((u64)__builtin_amdgcn_readfirstlane(t[1]) << 32) | (unsigned)__builtin_amdgcn_readfirstlane(t[0])) + 1;
  };
  auto do_RT = [&](int u, unsigned src, int f) {
    const int n2 = f >= 5 ? 1 : 0, c = f - n2 * 5;
    const int co = c < 4 ? (c >> 1) * 64 + p4 * 16 + (c & 1) * 8 : 128 + p4 * 8;
    const unsigned a = src + (16 * (g >> 1) + 4 * (g & 1) + q4) * 320 + n2 * 160 + co;
    const bf16x8 fr = cat8(tr_read(a), tr_read(a + 8 * 320));
    __builtin_amdgcn_raw_buffer_store_b128(__builtin_bit_cast(u32x4, fr), rw,
                                           WS_WT + (((u & 3) * 2 + n2) * NKS + (u >> 2)) * 5120 + c * 1024 + l16, 0, 16);
  };

  if (w == 8 || w == 9) {
    const int lw = w - 8;
    __builtin_amdgcn_s_setprio(3);
    const u32x4 sq = mk_srd(wqkv, D_ * 1920 * 2);
    const u32x4 s1 = mk_srd(w1, D_ * F_ * 2);
    unsigned xvo[2];
#pragma unroll
    for (int i = 0; i < 2; ++i) xvo[i] = (lane >> 3) * 1280 + (((lane & 7) ^ ((4 * i + (lane >> 4)) & 7)) * 16);
    const unsigned qvo = (lane >> 4) * 3840 + (((lane & 15) ^ ((lane >> 4) << 2)) * 16);
    const unsigned fvo = (lane >> 3) * 3456 + (((lane & 7) ^ (((lane >> 4) & 1) << 2)) * 16);
    const unsigned ord1[2] = {lf_pick<5, LFD1, 10, 0>((mt1 + jj) % 5), lf_pick<5, LFD1, 10, 1>((mt1 + jj) % 5)};
    unsigned l8 = lb + lw * 8192, l10 = lb + lw * 10240;
    unsigned xsb = (mt1 * 128 + 64 * lw) * 1280, qsb = 32 * lw * 3840 + jj * 256;
    PINS(l8); PINS(l10); PINS(xsb); PINS(qsb);
    auto issue_S = [&](auto tc) {
      constexpr int t = decltype(tc)::value;
      const int st = nib(ord1[t >> 3], t & 7);
      const unsigned xs = xsb + st * 128, qs = qsb + st * (64 * 3840);
      static_for<8>([&](auto nc) { constexpr int n = decltype(nc)::value; dma1<APART(t) + n * 1024, n * 8 * 1280, false>(sx, l8, xvo[n & 1], xs); });
      static_for<8>([&](auto nc) { constexpr int n = decltype(nc)::value; dma1<WPART(t) + n * 1024, n * 4 * 3840, false>(sq, l8, qvo, qs); });
    };
    if (!spare) { issue_S(IC<0>{}); issue_S(IC<1>{}); }
    const unsigned ordA = lf_pick<4, LFD2, 7, 0>(((mt2 & 3) + nt) & 3), ordB = lf_pick<4, LFD2, 6, 0>(((mt2 & 3) + nt) & 3),
                   ordS = lf_pick<4, LFD2, 4, 0>(((mt2 & 3) + nt) & 3);
    unsigned asb = WS_ACT + (mt2 * 4 + 2 * lw) * NKS * 1024, bsb = WS_WT + (nt * NKS + 2 * lw) * 5120;
    PINS(asb); PINS(bsb);
    auto issue_F = [&](auto uc) {
      constexpr int u = decltype(uc)::value;
      const int st = nib(ord1[(u % 5) >> 2], ((2 * (u % 5)) & 7) + lw);
      const unsigned fs = st * (64 * 3456) + (u < 5 ? chA : chB) * 128;
      static_for<8>([&](auto nc) { constexpr int n = decltype(nc)::value; dma1<APART(10 + u) + n * 1024, n * 8 * 3456, false>(s1, l8, fvo, fs); });
    };
    auto p2_stage = [&](int p) {
      if (p < 7) return nib(ordA, p);
      if (p < 13) return 7 + nib(ordB, p - 7);
      if (p >= 14 && p < 18) return 14 + nib(ordS, p - 14);
      return p;
    };
    auto issue_A = [&](auto pc) {
      constexpr int p = decltype(pc)::value, nk = p == NP - 1 ? 2 : 4;
      const unsigned src = asb + p2_stage(p) * 4096;
      dma_run<nk, APART(p + 2), 0>(sw, l8, l16, src);
      dma_run<nk, APART(p + 2) + 4096, NKS * 1024>(sw, l8, l16, src);
    };
    auto issue_B = [&](auto pc) {
      constexpr int p = decltype(pc)::value;
      if constexpr (p == NP - 1) {
        const unsigned l5 = lb + lw * 5120, src = WS_WT + (nt * NKS + 72 + lw) * 5120;
        dma_run<4, BPART(p), 0>(sw, l5, l16, src);
        dma_run<1, BPART(p) + 4096, 4096>(sw, l5, l16, src);
      } else {
        const unsigned src = bsb + p2_stage(p) * 20480;
        dma_run<4, BPART(p), 0>(sw, l10, l16, src);
        dma_run<4, BPART(p) + 4096, 4096>(sw, l10, l16, src);
        dma_run<2, BPART(p) + 8192, 8192>(sw, l10, l16, src);
      }
    };
    auto issue_img1 = [&](int sec, int rbl, unsigned dst) {
      int rbg = (R0 >> 4) - 1 + rbl; rbg = rbg < 0 ? 0 : rbg;
      const unsigned src = WS_QKV + (rbg * 60 + sec * 20 + ho * 5) * 1024;
      const unsigned ivo = g * 256 + (((m & 7) << 1) | (((m >> 3) ^ rbl ^ g) & 1)) * 16;
      unsigned d = lb + dst + rbl * 5120;
      PINS(d);
      dma_run<4, 0, 0>(sw, d, ivo, src);
      dma_run<1, 4096, 4096>(sw, d, ivo, src);
    };
    auto issue_img = [&]() {
      const unsigned dst = APART(0) + lw * 16384;
      issue_img1(1 + lw, 0, dst); issue_img1(1 + lw, 1, dst); issue_img1(1 + lw, 2, dst);
      issue_img1(0, 1 + lw, L_QIMG - 5120);
    };
    if (!spare) {
      static_for<21>([&](auto tc) {
        constexpr int T = decltype(tc)::value;
        if constexpr (wait_L(T) < 63) WAITV(wait_L(T));
        BAR();
        if constexpr (T <= 2) issue_S(IC<T + 2>{});
        else if constexpr (T == 3) { issue_S(IC<5>{}); issue_S(IC<6>{}); }
        else if constexpr (T <= 6) issue_S(IC<T + 3>{});
        else if constexpr (T <= 16) issue_F(IC<T - 7>{});
        if constexpr (T == T_C5) issue_B(IC<0>{});
        if constexpr (T == T_C5 + 1) issue_B(IC<1>{});
        if constexpr (T == T_C1) issue_img();
      });
    } else {
      BAR(); BAR(); BAR();
      static_for<2>(issue_B);
      BAR();
      issue_img();
    }
    BAR();
    WAITV(0);
    BAR();
    issue_A(IC<0>{}); issue_A(IC<1>{}); issue_B(IC<2>{}); issue_B(IC<3>{});
    static_for<NP>([&](auto pc) {
      constexpr int p = decltype(pc)::value;
      constexpr int n = p == 0 ? 28 : p == 1 ? 36 : p == 2 ? 26 : (p == 5 || p == 11) ? 28 : (p == 6 || p == 7) ? 41 :
                        p <= 15 ? 36 : p == 16 ? 27 : p == 17 ? 9 : 0;
      WAITV(n);
      BAR();
      if constexpr (p == 5 || p == 11) issue_A(IC<p + 2>{});
      if constexpr (p == 5) {
        u32x4 v[5];
#pragma unroll
        for (int P = 0; P < 5; ++P)
          v[P] = ldsr<u32x4>(lb + L_QIMG + (lw * 5 + P) * 1024 + g * 256 + ((m >> 1) | (((m ^ (lw + 1) ^ g) & 1) << 3)) * 16);
#pragma unroll
        for (int P = 0; P < 5; ++P)
          __builtin_amdgcn_raw_buffer_store_b128(v[P], rw, WS_ACT + (((R0 >> 4) + lw) * NKS + NKS_G + ho * 5 + P) * 1024 + l16, 0, 16);
      }
      if constexpr (p == 0) { issue_A(IC<2>{}); issue_A(IC<3>{}); }
      else if constexpr (p == 4 || p == 10) issue_B(IC<p + 3>{});
      else if constexpr (p + 3 < NP) { issue_A(IC<p + 3>{}); issue_B(IC<p + 3>{}); }
    });
    return;
  }

  if (w >= 10) {
    const int xw = w - 10;
    __builtin_amdgcn_s_setprio(3);
    const u32x4 s2 = mk_srd(w2, F_ * D_ * 2);
    const u32x4 so = mk_srd(wo, D_ * D_ * 2);
    auto issue_RT = [&](int u, unsigned dst) {
      const int ks = u >> 2;
      const unsigned c0 = (ks < NKS_G ? ks : ks - NKS_G) * 40960 + (u & 3) * 320;
#pragma unroll
      for (int i5 = 0; i5 < 5; ++i5) {
        const int i = xw * 5 + i5, q = i * 64 + lane, row = q / 20, pc = q - row * 20;
        if (ks < NKS_G) dma16(s2, dst + i * 1024, row * 1280 + pc * 16, c0); else dma16(so, dst + i * 1024, row * 1280 + pc * 16, c0);
      }
    };
    auto issue_CU = [&](int i0) {
      if (hasCU) {
        const bool cq = cu_ck < 30;
        const u32x4 sc = mk_srd(cq ? wqkv : w1, cq ? D_ * 1920 * 2 : D_ * F_ * 2);
        const unsigned str = cq ? 3840u : 3456u;
        const unsigned c0 = (cq ? cu_ck : cu_ck - 30) * 128u + ((rem & 3) * 160u + xw * 80u) * str;
        const int r8 = lane >> 3, pc = lane & 7;
#pragma unroll
        for (int i = i0; i < i0 + 5; ++i) {
          const int sv = ((r8 >> 1) & 1) | ((i & 1) << 1);
          const int n = xw * 10 + i;
          dma16(sc, lb + (n < 16 ? L_CU + n * 1024 : L_CS + (n - 16) * 1024),
                r8 * str + ((((pc >> 1) ^ sv) << 1) | (pc & 1)) * 16, c0 + i * 8 * str);
        }
      }
    };
    if (spare) {
#pragma unroll
      for (int it = 0; it < 4; ++it) { const int u = 240 + wk + it * 16; if (u < 296) issue_RT(u, lb + it * 10240); }
    }
    load_tag();
    auto put_flag = [&](unsigned off) {
      if (xw == 0) {
        const u32x4 f = {(unsigned)tag, (unsigned)(tag >> 32), (unsigned)tag, (unsigned)(tag >> 32)};
        __builtin_amdgcn_raw_buffer_store_b128(f, rw, lane < 8 ? off + l16 : OOB, 0, 16);
      }
    };
    auto wait_flags = [&](u32x2 v, bool fresh, unsigned voff) {
      const unsigned tl = (unsigned)tag, th = (unsigned)(tag >> 32);
#pragma unroll 1
      for (unsigned spins = 0; spins < SPIN_CAP; ++spins) {
        if (!fresh) ld8c(v, sw, voff, 0);
        WAITV(0);
        PIN(v);
        if (__all(v[0] == tl && v[1] == th)) break;
        fresh = false;
      }
    };
    const int l15 = (lane & 15) < 15 ? (lane & 15) : 0;
    const unsigned fo5 = WS_E5 + (xw ? 240 + (lane & 15) : (lane < 60 ? lane : 0) * 4 + (nt >> 1)) * 128;
    const unsigned fo1 = WS_E1 + ((xw ? (R0 >> 7) : (R0 > 0 ? (R0 - 16) >> 7 : 0)) * 16 + l15) * 128;
    const unsigned fo2 = WS_E2 + ((mt2 >> 1) * 16 + l15) * 128;
    const unsigned fo3 = WS_E3 + ((mt2 >> 1) * 16 + l15) * 128, fo4 = WS_E4 + (mt2 * 8 + (lane & 7)) * 128;
    u32x2 fv = {0u, 0u};
    auto put_stats = [&]() {
      if (xw == 1) {
        const int srow = spare ? wk * 128 + 120 : mt1 * 128 + 8 * jj;
        const u32x2 gr = {ldsr<unsigned>(lb + L_SX + (lane & 15) * 4), (unsigned)tag};
        __builtin_amdgcn_raw_buffer_store_b64(gr, rw, lane < 16 ? WS_STAT + srow * 16 + lane * 8 : OOB, 0, 16);
      }
    };
    if (!spare) {
      u32x4 c4[4], s4;
      const unsigned co = xw ? (1920 + (lane < 32 ? chA : chB) * 64) * 8 + (lane & 31) * 16 : jj * 1024 + l16;
      auto load_gran = [&]() {
#pragma unroll
        for (int k = 0; k < 4; ++k) ld16c(c4[k], sw, co, WS_CS + k * 3648 * 8);
        ld16c(s4, sw, l16, WS_STAT + mt1 * 2048 + xw * 1024);
      };
      static_for<21>([&](auto tc) {
        constexpr int T = decltype(tc)::value;
        if constexpr (T == 3 || T == 7) WAITV(0);
        if constexpr (T == T_C5) wait_flags(fv, true, fo5);
        if constexpr (T == 10) {
          const unsigned tag32 = (unsigned)tag;
#pragma unroll 1
          for (unsigned spins = 0; spins < SPIN_CAP; ++spins) {
            WAITV(0);
            PIN(c4[0]); PIN(c4[1]); PIN(c4[2]); PIN(c4[3]); PIN(s4);
            bool ok = s4[1] == tag32 && s4[3] == tag32;
#pragma unroll
            for (int k = 0; k < 4; ++k) ok = ok && c4[k][1] == tag32 && c4[k][3] == tag32;
            if (__all(ok)) break;
            load_gran();
          }
          const f32x2 cs2 = {(__uint_as_float(c4[0][0]) + __uint_as_float(c4[1][0])) + (__uint_as_float(c4[2][0]) + __uint_as_float(c4[3][0])),
                             (__uint_as_float(c4[0][2]) + __uint_as_float(c4[1][2])) + (__uint_as_float(c4[2][2]) + __uint_as_float(c4[3][2]))};
          ldsw<f32x2>(lb + L_CS + xw * 512 + lane * 8, cs2);
          ldsw<u32x2>(lb + L_ST + xw * 512 + lane * 8, (u32x2){s4[0], s4[2]});
        }
        if constexpr (T == T_C1) wait_flags(fv, true, fo1);
        BAR();
        if constexpr (T == T_E5) put_flag(WS_E5 + t1 * 128);
        if constexpr (T == T_E1) put_flag(WS_E1 + (mt1 * 16 + jj) * 128);
        if constexpr (T == T_E2) put_flag(WS_E2 + (mt1 * 16 + jj) * 128);
        if constexpr (T == 0) { put_stats(); issue_CU(0); }
        if constexpr (T == 1) issue_CU(5);
        if constexpr (T == 5) issue_RT(t1, lb + L_QIMG);
        if constexpr (T == 3) {
          const u32x4 o4 = {0x3F803F80u, 0x3F803F80u, 0x3F803F80u, 0x3F803F80u};
          const bf16x8 ones = __builtin_bit_cast(bf16x8, o4);
          const int sv = ((q4 >> 1) & 1) | ((g & 1) << 1);
#pragma unroll
          for (int c = 0; c < 2; ++c) {
            const unsigned p = lb + (8 * g + q4) * 128 + (((2 * xw + c) ^ sv) * 32) + p4 * 8;
            f32x4 a = {0.f, 0.f, 0.f, 0.f};
#pragma unroll
            for (int ks = 0; ks < 5; ++ks) {
              const unsigned pk = p + (ks < 4 ? L_CU + ks * 4096 : L_CS);
              a = MFMA(cat8(tr_read(pk), tr_read(pk + 512)), ones, a);
            }
            const float val = m == 0 ? a[0] : m == 1 ? a[1] : m == 2 ? a[2] : a[3];
            const u32x2 g2 = {__float_as_uint(val), (unsigned)tag};
            const unsigned dst = WS_CS + ((rem & 3) * 3648 + cu_ck * 64 + (2 * xw + c) * 16 + 4 * g + m) * 8;
            __builtin_amdgcn_raw_buffer_store_b64(g2, rw, (hasCU && m < 4) ? dst : OOB, 0, 16);
          }
        }
        if constexpr (T == 7) { do_RT(t1, lb + L_QIMG, xw * 5); do_RT(t1, lb + L_QIMG, xw * 5 + 1); do_RT(t1, lb + L_QIMG, xw * 5 + 2); }
        if constexpr (T == 8) { do_RT(t1, lb + L_QIMG, xw * 5 + 3); do_RT(t1, lb + L_QIMG, xw * 5 + 4); load_gran(); }
        if constexpr (T == T_C5 - 2) ld8c(fv, sw, fo5, 0);
        if constexpr (T == T_C1 - 2) ld8c(fv, sw, fo1, 0);
      });
    } else {
      WAITV(0);
      BAR();
      put_stats();
      BAR();
      put_flag(WS_E5 + (240 + wk) * 128);
      wait_flags(fv, false, fo5);
      if (wk == 0) {
        wait_flags(fv, false, WS_E5 + (xw * 128 + lane) * 128);
        wait_flags(fv, false, WS_E5 + (xw * 128 + 64 + lane) * 128);
      }
      BAR();
      if (wk == 0) put_flag(WS_M);
      wait_flags(fv, false, fo1);
      BAR();
    }
    BAR();
    wait_flags(fv, false, fo2);
    BAR();
    if (xw == 1) return;
    static_for<P_C4 + 1>([&](auto pc) {
      constexpr int p = decltype(pc)::value;
      if constexpr (p == P_C3) wait_flags(fv, true, fo3);
      if constexpr (p == P_C4) wait_flags(fv, true, fo4);
      BAR();
      if constexpr (p == P_E3) { if (!spare) put_flag(WS_E3 + (mt1 * 16 + jj) * 128); }
      if constexpr (p == P_E4) put_flag(WS_E4 + (mt2 * 8 + nt) * 128);
      if constexpr (p == P_C3 - 1) ld8c(fv, sw, fo3, 0);
      if constexpr (p == P_C4 - 1) ld8c(fv, sw, fo4, 0);
    });
    return;
  }

  {
    const int srow = (spare ? wk * 128 + 120 : mt1 * 128 + 8 * jj) + w;
    u32x4 ra, rb2;
    ld16(ra, sx, l16, srow * 1280);
    ld16(rb2, sx, lane < 16 ? 1024 + l16 : OOB, srow * 1280);
    WAITV(0);
    PIN(ra); PIN(rb2);
    float v[16];
#pragma unroll
    for (int j = 0; j < 4; ++j) {
      v[2 * j] = bflo(ra[j]); v[2 * j + 1] = bfhi(ra[j]);
      v[8 + 2 * j] = bflo(rb2[j]); v[8 + 2 * j + 1] = bfhi(rb2[j]);
    }
    float s = 0.f;
#pragma unroll
    for (int j = 0; j < 16; ++j) s += v[j];
    const float mean = wave_sum(s) * (1.f / D_);
    float qa = 0.f, qb = 0.f;
#pragma unroll
    for (int j = 0; j < 8; ++j) { const float d = v[j] - mean; qa += d * d; const float e = v[8 + j] - mean; qb += e * e; }
    const float qq = wave_sum(qa + (lane < 16 ? qb : 0.f));
    const float rstd = __builtin_amdgcn_rsqf(qq * (1.f / D_) + 1e-5f);
    if (lane == 0) ldsw<f32x2>(lb + L_SX + w * 8, (f32x2){rstd, -mean * rstd});
    if (spare) {
      BAR();
      const int u = 240 + wk + (w >> 1) * 16;
      if (u < 296) {
#pragma unroll
        for (int c = 0; c < 5; ++c) do_RT(u, lb + (w >> 1) * 10240, (w & 1) * 5 + c);
      }
    }
  }
  const int rg = w & 3, kp1 = w >> 2;
  const int m32 = lane & 31, hh = lane >> 5;
  f32x16 accf[2][2];
#pragma unroll
  for (int r = 0; r < 16; ++r) { accf[0][0][r] = 0.f; accf[0][1][r] = 0.f; accf[1][0][r] = 0.f; accf[1][1][r] = 0.f; }
  auto lane_off = [&](unsigned blk) { return (mt1 * 8 + rg * 2 + (m32 >> 4)) * blk + hh * 256 + (m32 & 15) * 16; };
  auto quad = [](const f32x16& a, int i) { return (f32x4){a[4 * i], a[4 * i + 1], a[4 * i + 2], a[4 * i + 3]}; };
  auto epi_blk = [&](const f32x16& a, int csoff, bool act, unsigned dst) {
    const f32x2 stt = ldsr<f32x2>(lb + L_ST + (rg * 32 + m32) * 8);
    f32x4 y[4];
#pragma unroll
    for (int i = 0; i < 4; ++i) y[i] = quad(a, i) * stt[0] + ldsr<f32x4>(lb + L_CS + (csoff + 8 * i + 4 * hh) * 4) * stt[1];
    if (act) {
      f32x4 e[4];
#pragma unroll
      for (int k = 0; k < 4; ++k)
#pragma unroll
        for (int r = 0; r < 4; ++r) e[k][r] = __builtin_amdgcn_exp2f(-1.44269504089f * y[k][r]);
#pragma unroll
      for (int k = 0; k < 4; ++k)
#pragma unroll
        for (int r = 0; r < 4; ++r) y[k][r] *= __builtin_amdgcn_rcpf(1.f + e[k][r]);
    }
    const u32x4 o0 = {pack2(y[0][0], y[0][1]), pack2(y[0][2], y[0][3]), pack2(y[1][0], y[1][1]), pack2(y[1][2], y[1][3])};
    const u32x4 o1 = {pack2(y[2][0], y[2][1]), pack2(y[2][2], y[2][3]), pack2(y[3][0], y[3][1]), pack2(y[3][2], y[3][3])};
    __builtin_amdgcn_raw_buffer_store_b128(o0, rw, dst, 0, 16);
    __builtin_amdgcn_raw_buffer_store_b128(o1, rw, dst + 512, 0, 16);
  };
  if (!spare) {
    bf16x8 xf[10][2], xt[2];
    f32x16 accq[2];
    f32x2 es; f32x4 ec, ee, ex, ey; u32x4 eo;
    bf16x4 wl[2][2], wh[2][2];
    unsigned xrd[4], qrd[2][2], frd[2];
    {
      const int nb = (lane >> 4) & 1;
#pragma unroll
      for (int k = 0; k < 4; ++k) {
        const int ks = k ^ (2 * kp1);
        xrd[k] = lb + APART(0) + (rg * 32 + m32) * 128 + (((2 * ks + hh) ^ ((m32 >> 1) & 7)) * 16); PIN(xrd[k]);
      }
#pragma unroll
      for (int k = 0; k < 2; ++k)
#pragma unroll
        for (int c = 0; c < 2; ++c) {
          qrd[k][c] = lb + (32 * (k ^ kp1) + 8 * hh + q4) * 256 + (((kp1 * 2 + c) ^ q4) * 64) + nb * 32 + p4 * 8;
          PIN(qrd[k][c]);
        }
    }
#pragma unroll
    for (int r = 0; r < 16; ++r) { accq[0][r] = 0.f; accq[1][r] = 0.f; }
    auto load_piece = [&](auto ic, auto rc) {
      constexpr int i = decltype(ic)::value, r = decltype(rc)::value, T = i / 4, pos = i & 3, c = r >> 1;
      if constexpr (T < 10) {
        if constexpr (r == 0) {
          if constexpr (pos < 2) xf[T][pos] = ldsr<bf16x8>(xrd[pos] + (APART(T) - APART(0)));
          else xt[pos & 1] = ldsr<bf16x8>(xrd[pos] + (APART(T) - APART(0)));
        }
        constexpr int wo_ = WPART(T) + (pos & 1) * 16 * 256 + (r & 1) * 4 * 256;
        if constexpr ((r & 1) == 0) wl[i & 1][c] = tr_read(qrd[pos >> 1][c] + wo_);
        else wh[i & 1][c] = tr_read(qrd[pos >> 1][c] + wo_);
      } else if constexpr (T < 20) {
        constexpr int wo_ = APART(T) - APART(0) + (pos >> 1) * 64 * 128 + (pos & 1) * 16 * 128 + (r & 1) * 4 * 128;
        if constexpr ((r & 1) == 0) wl[i & 1][c] = tr_read(frd[c] + wo_);
        else wh[i & 1][c] = tr_read(frd[c] + wo_);
      }
    };
    BAR();
    static_for<4>([&](auto rc) { load_piece(IC<0>{}, rc); });
    static_for<80>([&](auto ic) {
      constexpr int i = decltype(ic)::value, T = i / 4, ps = i & 3;
      constexpr int nx = i + 1, Tn = nx / 4, pos = nx & 3;
      if constexpr (pos == 0) {
        if constexpr (Tn == T_E1 || Tn == T_E2) WAITV(0);
        BAR();
      }
      if constexpr (i == 38) {
        const int nb = (lane >> 4) & 1;
#pragma unroll
        for (int c = 0; c < 2; ++c) {
          frd[c] = lb + APART(0) + (32 * kp1 + 8 * hh + q4) * 128 + (((c ^ kp1) ^ ((q4 >> 1) & 1)) * 64) + nb * 32 + p4 * 8;
          PIN(frd[c]);
        }
      }
      static_for<2>([&](auto cc) {
        constexpr int c = decltype(cc)::value;
        const bf16x8 wfr = cat8(wl[i & 1][c], wh[i & 1][c]);
        if constexpr (T < 10) accq[c] = MFMA32(wfr, ps < 2 ? xf[T][ps & 1] : xt[ps & 1], accq[c]);
        else accf[(T - 10) / 5][c] = MFMA32(wfr, xf[((T - 10) % 5) * 2 + (ps >> 1)][ps & 1], accf[(T - 10) / 5][c]);
        SCHED();
        load_piece(IC<nx>{}, IC<2 * c>{});
        load_piece(IC<nx>{}, IC<2 * c + 1>{});
        constexpr int sl = 2 * i + c;
        if constexpr (sl >= 80 && sl < 80 + 16) {
          constexpr int k = sl - 80, qd = k >> 2, st = k & 3;
          if constexpr (st == 0) {
            if constexpr (qd == 0) es = ldsr<f32x2>(lb + L_ST + (rg * 32 + m32) * 8);
            ec = ldsr<f32x4>(lb + L_CS + (64 * kp1 + 32 + 8 * qd + 4 * hh) * 4);
          } else if constexpr (st == 2) {
            ey = quad(accq[1], qd) * es[0] + ec * es[1];
            eo[2 * (qd & 1)] = pack2(ey[0], ey[1]); eo[2 * (qd & 1) + 1] = pack2(ey[2], ey[3]);
            if constexpr (qd & 1)
              __builtin_amdgcn_raw_buffer_store_b128(eo, rw, WS_QKV + (jj * 4 + kp1 * 2 + 1) * 1024 + (qd >> 1) * 512 + lane_off(60 * 1024), 0, 16);
          }
        }
        if constexpr (sl >= 121 && sl < 125)
          ldsw<f32x4>(lb + L_EX + w * 4096 + (sl - 121) * 1024 + l16, quad(accf[0][1], sl - 121));
        if constexpr (sl >= 126 && sl < 128 + 16) {
          constexpr int k = sl - 128, qd = k >= 0 ? k >> 2 : -1, st = k >= 0 ? k & 3 : 9;
          if constexpr (sl == 126 || (st == 1 && qd < 3)) {
            if constexpr (sl == 126) es = ldsr<f32x2>(lb + L_ST + (rg * 32 + m32) * 8);
            ec = ldsr<f32x4>(lb + L_CS + (128 + 32 * kp1 + 8 * (qd + 1) + 4 * hh) * 4);
            ex = ldsr<f32x4>(lb + L_EX + (w ^ 4) * 4096 + (qd + 1) * 1024 + l16);
          }
          if constexpr (st == 0) {
            ey = (quad(accf[0][0], qd) + ex) * es[0] + ec * es[1];
            ee = ey * -1.44269504089f;
          } else if constexpr (st == 1) {
#pragma unroll
            for (int r = 0; r < 4; ++r) ee[r] = __builtin_amdgcn_exp2f(ee[r]);
          } else if constexpr (st == 2) {
            ee += 1.f;
#pragma unroll
            for (int r = 0; r < 4; ++r) ee[r] = __builtin_amdgcn_rcpf(ee[r]);
          } else if constexpr (st == 3) {
            ey *= ee;
            eo[2 * (qd & 1)] = pack2(ey[0], ey[1]); eo[2 * (qd & 1) + 1] = pack2(ey[2], ey[3]);
            if constexpr (qd & 1)
              __builtin_amdgcn_raw_buffer_store_b128(eo, rw, WS_ACT + (chA * 2 + kp1) * 1024 + (qd >> 1) * 512 + lane_off(NKS * 1024), 0, 16);
          }
        }
        SCHED();
      });
      if constexpr (i == 39) { epi_blk(accq[0], 64 * kp1, false, WS_QKV + (jj * 4 + kp1 * 2) * 1024 + lane_off(60 * 1024)); SCHED(); }
    });
  } else {
    WAITV(0);
    BAR(); BAR(); BAR();
  }
#pragma unroll
  for (int k = 0; k < 4; ++k) ldsw<f32x4>(lb + L_EX + w * 4096 + k * 1024 + l16, quad(accf[1][1], k));
  BAR();
  {
    f32x16 t = accf[1][0];
#pragma unroll
    for (int k = 0; k < 4; ++k) {
      const f32x4 e = ldsr<f32x4>(lb + L_EX + (w ^ 4) * 4096 + k * 1024 + l16);
#pragma unroll
      for (int r = 0; r < 4; ++r) t[4 * k + r] += e[r];
    }
    epi_blk(t, 192 + 32 * kp1, true, hasB ? WS_ACT + (chB * 2 + kp1) * 1024 + lane_off(NKS * 1024) : OOB);
  }
  BAR();
  if (w >= 4) {
    __builtin_amdgcn_s_setprio(1);
    const unsigned kimg = lb + APART(0), qimg = lb + L_QIMG - 5120;
    constexpr int VO = APART(1) - APART(0);
    auto ioff = [](int pc, int i, int rbl) {
      return (pc >> 3) * 1024 + (((pc >> 1) & 2) | (pc & 1)) * 256 + (i | (((rbl ^ pc) & 1) << 3)) * 16 + ((pc >> 1) & 1) * 8;
    };
    const int hq = m >> 3, hv = g >> 1, iv = (4 * g + q4) & 7;
    bf16x8 qf[4], kc[4], kp[4], v0[4], v1[4];
    unsigned c0[2], c1[2];
#pragma unroll
    for (int h = 0; h < 2; ++h) {
      const int pc0 = 10 * (w - 4) + 5 * h;
      c0[h] = (1 + hq) * 5120 + ioff(pc0 + g, m & 7, 1 + hq); c1[h] = (1 + hq) * 5120 + ioff(pc0 + 4, m & 7, 1 + hq);
      const unsigned r0 = ioff(pc0 + g, m & 7, 0), r1 = ioff(pc0 + 4, m & 7, 0);
      const unsigned d0 = (1 + hv) * 5120 + ioff(pc0 + p4, iv, 1 + hv), d1 = (1 + hv) * 5120 + ioff(pc0 + 4, iv, 1 + hv);
      const unsigned e0 = ioff(pc0 + p4, iv, 0), e1 = ioff(pc0 + 4, iv, 0);
#pragma unroll
      for (int p = 0; p < 2; ++p) {
        const unsigned X = p * 128; const int u = 2 * h + p;
        kc[u] = cat8(ldsr<bf16x4>(kimg + (c0[h] ^ X)), ldsr<bf16x4>(kimg + (c1[h] ^ X)));
        kp[u] = cat8(ldsr<bf16x4>(kimg + (r0 ^ X)), ldsr<bf16x4>(kimg + (r1 ^ X)));
        v0[u] = cat8(tr_read(kimg + VO + (e0 ^ X)), tr_read(kimg + VO + (d0 ^ X)));
        v1[u] = cat8(tr_read(kimg + VO + (e1 ^ X)), tr_read(kimg + VO + (d1 ^ X)));
      }
    }
    BAR();
#pragma unroll
    for (int u = 0; u < 4; ++u) {
      const bf16x4 q0 = ldsr<bf16x4>(qimg + (c0[u >> 1] ^ ((u & 1) * 128)));
      u32x2 q1u = ldsr<u32x2>(qimg + (c1[u >> 1] ^ ((u & 1) * 128)));
      q1u[0] = g == 0 ? q1u[0] : 0u; q1u[1] = g == 0 ? q1u[1] : 0u;
      qf[u] = cat8(q0, __builtin_bit_cast(bf16x4, q1u));
    }
    f32x4 mp, mc;
#pragma unroll
    for (int r = 0; r < 4; ++r) {
      const int kj = 4 * g + r;
      mp[r] = (R0 > 0 && kj >= m + 9) ? 0.f : -1.0e30f;
      mc[r] = (kj <= m && m - kj <= 7) ? 0.f : -1.0e30f;
    }
    const float sc = 0.22360679775f * 1.44269504089f;
    const f32x4 z4 = {0.f, 0.f, 0.f, 0.f};
    f32x4 sp[4], su[4];
    float mx[4];
#pragma unroll
    for (int u = 0; u < 4; ++u) { sp[u] = MFMA(kp[u], qf[u], mp); su[u] = MFMA(kc[u], qf[u], mc); PIN(sp[u]); PIN(su[u]); }
    BAR();
#pragma unroll
    for (int u = 0; u < 4; ++u)
      mx[u] = fmaxf(fmaxf(fmaxf(sp[u][0], sp[u][1]), fmaxf(sp[u][2], sp[u][3])), fmaxf(fmaxf(su[u][0], su[u][1]), fmaxf(su[u][2], su[u][3])));
#pragma unroll
    for (int u = 0; u < 4; ++u) { mx[u] = -sc * max_g(mx[u]); PIN(mx[u]); }
    WAITV(0);
    BAR();
#pragma unroll
    for (int u = 0; u < 4; ++u) {
#pragma unroll
      for (int r = 0; r < 4; ++r) {
        sp[u][r] = __builtin_amdgcn_exp2f(sp[u][r] * sc + mx[u]);
        su[u][r] = __builtin_amdgcn_exp2f(su[u][r] * sc + mx[u]);
      }
      const f32x4 t = sp[u] + su[u];
      mx[u] = (t[0] + t[1]) + (t[2] + t[3]);
    }
#pragma unroll
    for (int u = 0; u < 4; ++u) { mx[u] = __builtin_amdgcn_rcpf(sum_g(mx[u])); PIN(mx[u]); }
    BAR();
    f32x4 o0[4], o1[4];
#pragma unroll
    for (int u = 0; u < 4; ++u) {
      sp[u] *= mx[u]; su[u] *= mx[u];
      const u32x4 pu = {pack2(sp[u][0], sp[u][1]), pack2(sp[u][2], sp[u][3]), pack2(su[u][0], su[u][1]), pack2(su[u][2], su[u][3])};
      const bf16x8 pf = __builtin_bit_cast(bf16x8, pu);
      o0[u] = MFMA(v0[u], pf, z4);
      o1[u] = MFMA(v1[u], pf, z4);
      PIN(o0[u]); PIN(o1[u]);
    }
    BAR();
#pragma unroll
    for (int u = 0; u < 4; ++u) {
      ldsw<u32x2>(qimg + (c0[u >> 1] ^ ((u & 1) * 128)), (u32x2){pack2(o0[u][0], o0[u][1]), pack2(o0[u][2], o0[u][3])});
      if (g == 0) ldsw<u32x2>(qimg + (c1[u >> 1] ^ ((u & 1) * 128)), (u32x2){pack2(o1[u][0], o1[u][1]), pack2(o1[u][2], o1[u][3])});
    }
    BAR();
    return;
  }
  {
    const int rh = w & 1, kp = (w >> 1) & 1, own = 2 * rh + kp, oth = 2 * rh + 1 - kp;
    f32x4 accg[2][5], accs[5];
#pragma unroll
    for (int c = 0; c < 5; ++c) {
      accg[0][c] = (f32x4){0.f, 0.f, 0.f, 0.f}; accg[1][c] = (f32x4){0.f, 0.f, 0.f, 0.f}; accs[c] = (f32x4){0.f, 0.f, 0.f, 0.f};
    }
    constexpr int NF = 3;
    bf16x8 af[NF][2], bfr[NF][5];
    u32x4 xr0, xr1; u32x2 xr2;
    const unsigned xo = (mt2 * 64 + own * 16 + m) * 1280 + nt * 160;
    unsigned ard[2], ars, brd[2], brs[2];
    ard[0] = lb + APART(0) + own * 4096 + kp * 1024 + l16; ard[1] = lb + APART(0) + oth * 4096 + kp * 1024 + l16;
    ars = lb + APART(0) + own * 4096 + l16;
    brd[0] = lb + kp * 5120 + l16; brd[1] = brd[0] + 40960; brs[0] = lb + l16; brs[1] = brs[0] + 40960;
    PIN(ard[0]); PIN(ard[1]); PIN(ars); PIN(brd[0]); PIN(brd[1]); PIN(brs[0]); PIN(brs[1]);
    constexpr int NJ = 47;
    auto jstage = [](int j) constexpr { return j < 27 ? j / 2 : (NKS_G + j - 27) / 4; };
    auto ldp = [&](auto jc, auto rc) {
      constexpr int j = decltype(jc)::value, r = decltype(rc)::value;
      if constexpr (j < NJ) {
        constexpr int p = jstage(j), kk = j < 27 ? 2 * (j & 1) : (NKS_G + j - 27) & 3;
        constexpr int hb = BPART(p) >= 40960;
        constexpr int oa = APART(p + 2) - APART(0) + kk * 1024, ob = BPART(p) - hb * 40960 + kk * 5120;
        if constexpr (j < 27) {
          if constexpr (r == 0) af[j % NF][0] = ldsr<bf16x8>(ard[0] + oa);
          else if constexpr (r == 2) af[j % NF][1] = ldsr<bf16x8>(ard[1] + oa);
          else if constexpr (r == 1) bfr[j % NF][0] = ldsr<bf16x8>(brd[hb] + ob);
          else if constexpr (r < 7) bfr[j % NF][r - 2] = ldsr<bf16x8>(brd[hb] + ob + (r - 2) * 1024);
        } else {
          if constexpr (r == 0) af[j % NF][0] = ldsr<bf16x8>(ars + oa);
          else if constexpr (r < 6) bfr[j % NF][r - 1] = ldsr<bf16x8>(brs[hb] + ob + (r - 1) * 1024);
        }
      }
    };
    const unsigned exw = lb + L_MISC + w * 3072 + l16, exr = lb + L_MISC + (w ^ 2) * 3072 + l16;
    auto top = [&](auto pc) {
      constexpr int p = decltype(pc)::value;
      if constexpr (p == P_E3) WAITV(0);
      BAR();
      if constexpr (p == 16) {
        ld16(xr0, sx, xo + g * 16, 0);
        ld16(xr1, sx, xo + 64 + g * 16, 0);
        ld8(xr2, sx, xo + 128 + g * 8, 0);
      }
      if constexpr (p == 15) {
#pragma unroll
        for (int c = 0; c < 3; ++c) accg[0][c] += ldsr<f32x4>(exr + c * 1024);
      }
      if constexpr (p == 16) {
#pragma unroll
        for (int c = 3; c < 5; ++c) ldsw<f32x4>(exw + (c - 3) * 1024, accg[1][c]);
      }
      if constexpr (p == 17) {
#pragma unroll
        for (int c = 3; c < 5; ++c) accg[0][c] += ldsr<f32x4>(exr + (c - 3) * 1024);
      }
    };
    top(IC<0>{});
    static_for<7>([&](auto rc) { ldp(IC<0>{}, rc); });
    static_for<7>([&](auto rc) { ldp(IC<1>{}, rc); });
    static_for<NJ>([&](auto jc) {
      constexpr int j = decltype(jc)::value;
      if constexpr (j + 2 < NJ && jstage(j + 2) != jstage(j + 1)) top(IC<jstage(j + 2)>{});
      if constexpr (j < 27) {
        static_for<10>([&](auto kc) {
          constexpr int k = decltype(kc)::value, c = k >> 1, a = k & 1;
          accg[a][c] = MFMA(bfr[j % NF][c], af[j % NF][a], accg[a][c]);
          SCHED();
          ldp(IC<j + 2>{}, kc);
          SCHED();
        });
      } else {
        static_for<5>([&](auto kc) {
          constexpr int c = decltype(kc)::value;
          accs[c] = MFMA(bfr[j % NF][c], af[j % NF][0], accs[c]);
          SCHED();
          ldp(IC<j + 2>{}, kc);
          if constexpr (c == 4) ldp(IC<j + 2>{}, IC<5>{});
          SCHED();
        });
      }
      if constexpr (j == 26) {
#pragma unroll
        for (int c = 0; c < 3; ++c) ldsw<f32x4>(exw + c * 1024, accg[1][c]);
      }
      if constexpr ((j >= 33 && j < 39) || (j >= 41 && j < 45)) {
        constexpr int e = j < 39 ? j - 33 : j - 35, c = e >> 1, r0 = (e & 1) * 2;
        accg[0][c][r0] = silu_f(accg[0][c][r0]); accg[0][c][r0 + 1] = silu_f(accg[0][c][r0 + 1]);
        SCHED();
      }
    });
    WAITV(0);
    PIN(xr0); PIN(xr1); PIN(xr2);
    const rsrc_t rout = mk_rsrc(out, S_ * D_ * 2);
    auto fin = [&](int c, unsigned lo, unsigned hi, unsigned& o0, unsigned& o1) {
      o0 = pack2(bflo(lo) + accs[c][0] * accg[0][c][0], bfhi(lo) + accs[c][1] * accg[0][c][1]);
      o1 = pack2(bflo(hi) + accs[c][2] * accg[0][c][2], bfhi(hi) + accs[c][3] * accg[0][c][3]);
    };
    unsigned o[10];
    fin(0, xr0[0], xr0[1], o[0], o[1]); fin(1, xr0[2], xr0[3], o[2], o[3]);
    fin(2, xr1[0], xr1[1], o[4], o[5]); fin(3, xr1[2], xr1[3], o[6], o[7]);
    fin(4, xr2[0], xr2[1], o[8], o[9]);
    __builtin_amdgcn_raw_buffer_store_b128((u32x4){o[0], o[1], o[2], o[3]}, rout, xo + g * 16, 0, 0);
    __builtin_amdgcn_raw_buffer_store_b128((u32x4){o[4], o[5], o[6], o[7]}, rout, xo + 64 + g * 16, 0, 0);
    __builtin_amdgcn_raw_buffer_store_b64((u32x2){o[8], o[9]}, rout, xo + 128 + g * 8, 0, 0);
  }
}

extern "C" void kernel_launch(void* const* d_in, const int* in_sizes, int n_in, void* d_out, int out_size,
                              void* d_ws, size_t ws_size, hipStream_t stream) {
  const u16* x = (const u16*)d_in[0];
  const u16* wqkv = (const u16*)d_in[1];
  const u16* wo = (const u16*)d_in[2];
  const u16* w1 = (const u16*)d_in[4];
  const u16* w2 = (const u16*)d_in[5];
  (void)hipFuncSetAttribute((const void*)fused_kernel, hipFuncAttributeMaxDynamicSharedMemorySize, L_TOTAL);
  fused_kernel<<<256, 768, L_TOTAL, stream>>>(x, wqkv, w1, w2, wo, (char*)d_ws, (u16*)d_out);
}
```
